```python
import torch

def _softmax(x, axis=-1):
    return torch.softmax(x, dim=axis)
def _gelu(x):
    return torch.nn.functional.gelu(x, approximate="tanh")
def _silu(x):
    return torch.nn.functional.silu(x)
def _sigmoid(x):
    return torch.sigmoid(x)
def _layernorm(x, axis=-1, eps=1e-5):
    m = x.mean(dim=axis, keepdim=True)
    v = x.var(dim=axis, keepdim=True, unbiased=False)
    return (x - m) / torch.sqrt(v + eps)
def _scatter_add(tgt, idx, src, axis=0):
    return tgt.clone().index_add_(axis, idx, src)
def _segment_sum(src, seg, n):
    out = torch.zeros((n,) + src.shape[1:], dtype=src.dtype)
    return out.index_add_(0, seg, src)
def _pad(x, pw):
    flat = tuple(v for pair in reversed(pw) for v in pair)
    return torch.nn.functional.pad(x, flat)

kernel_name = 'standalone__glu_gated_swiglu__sampling'

def setup_inputs():

    _w = (lambda a: (lambda t: t.to(torch.bfloat16) if t.is_floating_point() else t)(torch.as_tensor(a)))
    B, S, D, F = 32, 32, 512, 1792
    return {
        "x": _w((lambda *s: torch.randn(*s, dtype=torch.bfloat16) * 0.05)(B, S, D)),
        "w1": _w((lambda *s: torch.randn(*s, dtype=torch.bfloat16) * 0.05)(D, F) * (2.0 / D) ** 0.5),
        "w3": _w((lambda *s: torch.randn(*s, dtype=torch.bfloat16) * 0.05)(D, F) * (2.0 / D) ** 0.5),
        "w2": _w((lambda *s: torch.randn(*s, dtype=torch.bfloat16) * 0.05)(F, D) * (2.0 / F) ** 0.5),
    }

def reference(x, w1, w3, w2):
    gate = torch.einsum("bsd,df->bsf", x, w1)
    up = torch.einsum("bsd,df->bsf", x, w3)
    h = _silu(gate) * up
    out = torch.einsum("bsf,fd->bsd", h, w2)
    return out
```

```cpp
#include <hip/hip_runtime.h>
#include <hip/hip_bf16.h>

typedef short s16x4 __attribute__((ext_vector_type(4)));
typedef short s16x8 __attribute__((ext_vector_type(8)));
typedef __bf16 bf16x8 __attribute__((ext_vector_type(8)));
typedef __bf16 bf16x2 __attribute__((ext_vector_type(2)));
typedef float f32x4 __attribute__((ext_vector_type(4)));
typedef float f32x2 __attribute__((ext_vector_type(2)));
typedef unsigned u32x4 __attribute__((ext_vector_type(4)));
typedef unsigned u32x2 __attribute__((ext_vector_type(2)));
#define LAS __attribute__((address_space(3)))
#define GAS __attribute__((address_space(1)))

constexpr int M_ = 1024, D_ = 512, F_ = 1792;

__device__ __forceinline__ f32x4 mfma16(s16x8 a, s16x8 b, f32x4 c) {
  return __builtin_amdgcn_mfma_f32_16x16x32_bf16(__builtin_bit_cast(bf16x8, a), __builtin_bit_cast(bf16x8, b), c, 0, 0, 0);
}
__device__ __forceinline__ s16x8 tr_frag(LAS char* p0, LAS char* p1) {
  s16x4 a = __builtin_amdgcn_ds_read_tr16_b64_v4i16((LAS s16x4*)p0);
  s16x4 b = __builtin_amdgcn_ds_read_tr16_b64_v4i16((LAS s16x4*)p1);
  return __builtin_shufflevector(a, b, 0, 1, 2, 3, 4, 5, 6, 7);
}
__device__ __forceinline__ unsigned pack2(float a, float b) {
  f32x2 v = {a, b};
  bf16x2 r = __builtin_convertvector(v, bf16x2);
  return __builtin_bit_cast(unsigned, r);
}
__device__ __forceinline__ float swiglu(float g, float u) {
  float e = __builtin_amdgcn_exp2f(-1.44269504088896340736f * g);
  return g * u * __builtin_amdgcn_rcpf(1.0f + e);
}

__global__ __launch_bounds__(256, 1) void k_gateup(const unsigned short* __restrict__ x, const unsigned short* __restrict__ w1,
                                                   const unsigned short* __restrict__ w3, unsigned short* __restrict__ hbuf) {
  extern __shared__ __attribute__((aligned(16))) char smem[];
  LAS char* L = (LAS char*)smem;
  const int tid = threadIdx.x, lane = tid & 63, w = __builtin_amdgcn_readfirstlane(tid >> 6);
  const int bid = blockIdx.x, xcd = bid & 7, idx = bid >> 3;
  const int mt = 4 * (xcd & 1) + (idx & 3), ft = 7 * (xcd >> 1) + (idx >> 2);
  const int g = lane >> 4, li = lane & 15;
  {
    const int r = lane >> 3, cp = lane & 7;
#pragma unroll
    for (int c = 0; c < 8; ++c) {
#pragma unroll
      for (int i = 0; i < 4; ++i) {
        const int q = c * 16 + i * 4 + w;
        const int db = q >> 1, mat = q & 1;
        const int d = db * 8 + r;
        const int fz = ((d >> 1) & 1) | (((d >> 3) & 1) << 1);
        const int ch = (((cp >> 1) ^ fz) << 1) | (cp & 1);
        const unsigned short* src = (mat ? w3 : w1) + (size_t)d * F_ + ft * 64 + ch * 8;
        __builtin_amdgcn_global_load_lds((const GAS unsigned*)src, (LAS unsigned*)(L + mat * 65536 + db * 1024), 16, 0, 0);
      }
    }
  }
  s16x8 xf[2][16];
#pragma unroll
  for (int s = 0; s < 16; ++s)
#pragma unroll
    for (int ms = 0; ms < 2; ++ms)
      xf[ms][s] = *(const s16x8*)(x + (size_t)(mt * 128 + w * 32 + ms * 16 + li) * D_ + s * 32 + g * 8);
  asm volatile("s_waitcnt vmcnt(0)" ::: "memory");
  __syncthreads();
  f32x4 acc[2][4][2];
#pragma unroll
  for (int a = 0; a < 2; ++a)
#pragma unroll
    for (int b = 0; b < 4; ++b)
#pragma unroll
      for (int c = 0; c < 2; ++c) acc[a][b][c] = f32x4{0.f, 0.f, 0.f, 0.f};
  const int q4 = li >> 2, p4 = li & 3;
#pragma unroll
  for (int s = 0; s < 16; ++s) {
#pragma unroll
    for (int mat = 0; mat < 2; ++mat)
#pragma unroll
      for (int nt = 0; nt < 4; ++nt) {
        const int d0 = 32 * s + 8 * g + q4, d1 = d0 + 4;
        const int f0 = ((d0 >> 1) & 1) | (((d0 >> 3) & 1) << 1);
        const int f1 = ((d1 >> 1) & 1) | (((d1 >> 3) & 1) << 1);
        s16x8 wf = tr_frag(L + mat * 65536 + d0 * 128 + ((nt ^ f0) * 32) + p4 * 8, L + mat * 65536 + d1 * 128 + ((nt ^ f1) * 32) + p4 * 8);
#pragma unroll
        for (int ms = 0; ms < 2; ++ms) acc[mat][nt][ms] = mfma16(wf, xf[ms][s], acc[mat][nt][ms]);
      }
  }
#pragma unroll
  for (int ms = 0; ms < 2; ++ms)
#pragma unroll
    for (int k2 = 0; k2 < 2; ++k2) {
      u32x4 o;
      const f32x4 g0 = acc[0][2 * k2][ms], u0 = acc[1][2 * k2][ms], g1 = acc[0][2 * k2 + 1][ms], u1 = acc[1][2 * k2 + 1][ms];
      o.x = pack2(swiglu(g0[0], u0[0]), swiglu(g0[1], u0[1]));
      o.y = pack2(swiglu(g0[2], u0[2]), swiglu(g0[3], u0[3]));
      o.z = pack2(swiglu(g1[0], u1[0]), swiglu(g1[1], u1[1]));
      o.w = pack2(swiglu(g1[2], u1[2]), swiglu(g1[3], u1[3]));
      const int mt16 = mt * 8 + w * 2 + ms, ks = ft * 2 + k2;
      *(u32x4*)((char*)hbuf + ((size_t)(mt16 * 56 + ks) * 1024) + lane * 16) = o;
    }
}

__global__ __launch_bounds__(256, 1) void k_down(const unsigned short* __restrict__ hbuf, const unsigned short* __restrict__ w2,
                                                 unsigned short* __restrict__ out) {
  extern __shared__ __attribute__((aligned(16))) char smem[];
  LAS char* L = (LAS char*)smem;
  const int tid = threadIdx.x, lane = tid & 63, w = __builtin_amdgcn_readfirstlane(tid >> 6);
  const int bid = blockIdx.x, xcd = bid & 7, idx = bid >> 3;
  const int mt = 8 * (xcd >> 1) + (idx & 7), nt64 = 4 * (xcd & 1) + (idx >> 3);
  const int g = lane >> 4, li = lane & 15, q4 = li >> 2, p4 = li & 3;
  f32x4 acc[4][2];
#pragma unroll
  for (int b = 0; b < 4; ++b)
#pragma unroll
    for (int c = 0; c < 2; ++c) acc[b][c] = f32x4{0.f, 0.f, 0.f, 0.f};
  LAS char* LW = L + w * 28672;
  const int r = lane >> 3, cp = lane & 7;
#pragma unroll
  for (int rnd = 0; rnd < 2; ++rnd) {
    const int kbase = 448 * w + 224 * rnd;
#pragma unroll
    for (int i = 0; i < 28; ++i) {
      const int kl = i * 8 + r;
      const int fz = (kl >> 1) & 3;
      const int ch = (((cp >> 1) ^ fz) << 1) | (cp & 1);
      const unsigned short* src = w2 + (size_t)(kbase + kl) * D_ + nt64 * 64 + ch * 8;
      __builtin_amdgcn_global_load_lds((const GAS unsigned*)src, (LAS unsigned*)(LW + i * 1024), 16, 0, 0);
    }
    s16x8 hf[2][7];
#pragma unroll
    for (int s = 0; s < 7; ++s)
#pragma unroll
      for (int ms = 0; ms < 2; ++ms)
        hf[ms][s] = *(const s16x8*)((const char*)hbuf + (size_t)((mt * 2 + ms) * 56 + (kbase >> 5) + s) * 1024 + lane * 16);
    asm volatile("s_waitcnt vmcnt(0)" ::: "memory");
#pragma unroll
    for (int s = 0; s < 7; ++s)
#pragma unroll
      for (int nt = 0; nt < 4; ++nt) {
        const int k0 = 32 * s + 4 * g + q4, k1 = k0 + 16;
        s16x8 wf = tr_frag(LW + k0 * 128 + ((nt ^ ((k0 >> 1) & 3)) * 32) + p4 * 8, LW + k1 * 128 + ((nt ^ ((k1 >> 1) & 3)) * 32) + p4 * 8);
#pragma unroll
        for (int ms = 0; ms < 2; ++ms) acc[nt][ms] = mfma16(wf, hf[ms][s], acc[nt][ms]);
      }
    asm volatile("s_waitcnt lgkmcnt(0)" ::: "memory");
  }
  __syncthreads();
  LAS f32x4* red = (LAS f32x4*)L;
#pragma unroll
  for (int nt = 0; nt < 4; ++nt)
#pragma unroll
    for (int ms = 0; ms < 2; ++ms) red[(w * 8 + nt * 2 + ms) * 64 + g * 16 + ((li + 2 * (nt * 2 + (g >> 1))) & 15)] = acc[nt][ms];
  __syncthreads();
  {
    const int e = tid & 7, mrow = tid >> 3;
    const int nt = e >> 1, a = e & 1, ms = mrow >> 4, m = mrow & 15;
    f32x4 s0 = {0.f, 0.f, 0.f, 0.f}, s1 = {0.f, 0.f, 0.f, 0.f};
#pragma unroll
    for (int ww = 0; ww < 4; ++ww) {
      s0 += red[(ww * 8 + nt * 2 + ms) * 64 + (2 * a) * 16 + ((m + 2 * e) & 15)];
      s1 += red[(ww * 8 + nt * 2 + ms) * 64 + (2 * a + 1) * 16 + ((m + 2 * e) & 15)];
    }
    u32x4 o;
    o.x = pack2(s0[0], s0[1]);
    o.y = pack2(s0[2], s0[3]);
    o.z = pack2(s1[0], s1[1]);
    o.w = pack2(s1[2], s1[3]);
    *(u32x4*)(out + (size_t)(mt * 32 + mrow) * D_ + nt64 * 64 + e * 8) = o;
  }
}

extern "C" void kernel_launch(void* const* d_in, const int* in_sizes, int n_in, void* d_out, int out_size, void* d_ws, size_t ws_size,
                              hipStream_t stream) {
  const unsigned short* x = (const unsigned short*)d_in[0];
  const unsigned short* w1 = (const unsigned short*)d_in[1];
  const unsigned short* w3 = (const unsigned short*)d_in[2];
  const unsigned short* w2 = (const unsigned short*)d_in[3];
  unsigned short* hbuf = (unsigned short*)d_ws;
  (void)hipFuncSetAttribute((const void*)k_gateup, hipFuncAttributeMaxDynamicSharedMemorySize, 131072);
  (void)hipFuncSetAttribute((const void*)k_down, hipFuncAttributeMaxDynamicSharedMemorySize, 114688);
  k_gateup<<<224, 256, 131072, stream>>>(x, w1, w3, hbuf);
  k_down<<<256, 256, 114688, stream>>>(hbuf, w2, (unsigned short*)d_out);
}
```

```cpp
#include <hip/hip_runtime.h>
#include <hip/hip_bf16.h>

__global__ __launch_bounds__(512, 2) __attribute__((aligned(4096))) void swiglu_fused(const void* x, const void* w1, const void* w3, const void* w2, void* out, void* ws) {
  asm volatile(
      ".Lstart%=:\n\t"
      "s_load_dwordx8 s[16:23], %0, 0\n\t"
      "s_load_dwordx4 s[24:27], %0, 32\n\t"
      "v_lshrrev_b32 v2, 6, v0\n\t"
      "v_and_b32 v1, 63, v0\n\t"
      "s_and_b32 s53, %1, 7\n\t"
      "v_readfirstlane_b32 s52, v2\n\t"
      "s_lshr_b32 s54, %1, 3\n\t"
      "s_nop 1\n\t"
      "s_lshr_b32 s59, s52, 2\n\t"
      "s_or_b32 s59, s59, s54\n\t"
      "s_cmp_lg_u32 s59, 0\n\t"
      "s_cbranch_scc1 .Lnopref%=\n\t"
      "s_getpc_b64 s[74:75]\n\t"
      ".Lpc%=:\n\t"
      "s_sub_u32 s74, s74, .Lpc%=-.Lstart%=\n\t"
      "s_subb_u32 s75, s75, 0\n\t"
      "v_lshlrev_b32 v9, 6, v0\n\t"
      "v_min_u32 v9, .Lend%=-.Lstart%=-4, v9\n\t"
      "global_load_dword v20, v9, s[74:75]\n\t"
      ".Lnopref%=:\n\t"
      "s_lshr_b32 s59, s53, 2\n\t"
      "s_and_b32 s53, s53, 3\n\t"
      "s_lshl1_add_u32 s53, s53, s59\n\t"
      "s_and_b32 s59, s53, 1\n\t"
      "s_and_b32 s60, s54, 3\n\t"
      "s_lshl2_add_u32 s55, s59, s60\n\t"
      "s_lshr_b32 s59, s53, 1\n\t"
      "s_lshr_b32 s61, s54, 2\n\t"
      "s_mul_i32 s56, s59, 7\n\t"
      "s_add_u32 s56, s56, s61\n\t"
      "s_lshl3_add_u32 s57, s59, s61\n\t"
      "s_xor_b32 s58, s55, 0\n\t"
      "s_and_b32 s73, s53, 1\n\t"
      "s_xor_b32 s73, s73, 1\n\t"
      "s_bfe_u32 s63, s53, 0x10001\n\t"
      "s_lshl1_add_u32 s73, s73, s63\n\t"
      "s_lshl3_add_u32 s57, s73, s61\n\t"
      "s_lshr_b32 s63, s53, 2\n\t"
      "s_lshl2_add_u32 s58, s63, s60\n\t"
      "s_lshl_b32 s73, s73, 1\n\t"
      "s_cmp_lt_u32 s52, 4\n\t"
      "s_cbranch_scc1 .Lcompute%=\n\t"
      "s_add_u32 s63, s60, s61\n\t"
      "s_and_b32 s63, s63, 1\n\t"
      "s_lshl_b32 s63, s63, 2\n\t"
      "s_sub_u32 s62, s52, 4\n\t"
      "v_lshrrev_b32 v2, 3, v1\n\t"
      "v_and_b32 v3, 7, v1\n\t"
      "v_lshrrev_b32 v4, 1, v2\n\t"
      "v_xor_b32 v5, v3, v4\n\t"
      "v_lshlrev_b32 v6, 10, v2\n\t"
      "s_lshl_b32 s59, s62, 15\n\t"
      "v_add_u32 v7, s59, v6\n\t"
      "v_lshl_add_u32 v24, v5, 4, v7\n\t"
      "v_xor_b32 v25, 64, v24\n\t"
      "v_add_u32 v25, 0x2000, v25\n\t"
      "v_add_u32 v26, 0x4000, v24\n\t"
      "v_add_u32 v27, 0x4000, v25\n\t"
      "v_and_b32 v4, 1, v4\n\t"
      "v_lshlrev_b32 v4, 1, v4\n\t"
      "v_xor_b32 v4, v3, v4\n\t"
      "v_lshlrev_b32 v4, 4, v4\n\t"
      "s_mul_i32 s59, s62, 57344\n\t"
      "v_mul_u32_u24 v7, 3584, v2\n\t"
      "v_add3_u32 v28, v7, v4, s59\n\t"
      "v_xor_b32 v29, 64, v28\n\t"
      "v_add_u32 v29, 28672, v29\n\t"
      "s_lshl_b32 s79, s55, 17\n\t"
      "s_lshl_b32 s92, s56, 7\n\t"
      "s_lshl_b32 s69, s62, 12\n\t"
      "s_lshl_b32 s70, s62, 11\n\t"
      "s_mov_b32 s30, 1048576\n\t"
      "s_mov_b32 s31, 0x20000\n\t"
      "s_mov_b32 s86, 1835008\n\t"
      "s_mov_b32 s87, 0x20000\n\t"
      "s_mov_b32 s38, 1835008\n\t"
      "s_mov_b32 s39, 0x20000\n\t"
      "s_lshl_b32 s66, s63, 7\n\t"
      "s_add_u32 s66, s66, s79\n\t"
      "s_mul_i32 s67, s63, 229376\n\t"
      "s_add_u32 s67, s67, s92\n\t"
      "s_add_u32 s63, s63, 1\n\t"
      "s_and_b32 s63, s63, 7\n\t"
      "s_waitcnt lgkmcnt(0)\n\t"
      "s_mov_b32 s28, s16\n\t"
      "s_and_b32 s29, s17, 0xffff\n\t"
      "s_mov_b32 s84, s18\n\t"
      "s_and_b32 s85, s19, 0xffff\n\t"
      "s_mov_b32 s36, s20\n\t"
      "s_and_b32 s37, s21, 0xffff\n\t"
      "s_cmp_ge_u32 s54, 28\n\t"
      "s_cbranch_scc1 .Lldnoprod%=\n\t"
      "s_add_u32 m0, s70, 16384\n\t"
      "s_nop 0\n\t"
      "buffer_load_dwordx4 v28, s[84:87], s67 offen  lds\n\t"
      "s_add_u32 m0, s70, 24576\n\t"
      "s_nop 0\n\t"
      "buffer_load_dwordx4 v28, s[36:39], s67 offen  lds\n\t"
      "s_add_u32 m0, s70, 17408\n\t"
      "s_nop 0\n\t"
      "buffer_load_dwordx4 v29, s[84:87], s67 offen  lds\n\t"
      "s_add_u32 m0, s70, 25600\n\t"
      "s_nop 0\n\t"
      "buffer_load_dwordx4 v29, s[36:39], s67 offen  lds\n\t"
      "s_add_u32 m0, s69, 0\n\t"
      "s_nop 0\n\t"
      "buffer_load_dwordx4 v24, s[28:31], s66 offen  lds\n\t"
      "s_add_u32 m0, s69, 1024\n\t"
      "s_nop 0\n\t"
      "buffer_load_dwordx4 v25, s[28:31], s66 offen  lds\n\t"
      "s_add_u32 m0, s69, 2048\n\t"
      "s_nop 0\n\t"
      "buffer_load_dwordx4 v26, s[28:31], s66 offen  lds\n\t"
      "s_add_u32 m0, s69, 3072\n\t"
      "s_nop 0\n\t"
      "buffer_load_dwordx4 v27, s[28:31], s66 offen  lds\n\t"
      "s_lshl_b32 s66, s63, 7\n\t"
      "s_add_u32 s66, s66, s79\n\t"
      "s_mul_i32 s67, s63, 229376\n\t"
      "s_add_u32 s67, s67, s92\n\t"
      "s_add_u32 s63, s63, 1\n\t"
      "s_and_b32 s63, s63, 7\n\t"
      "v_and_b32 v4, 6, v2\n\t"
      "v_xor_b32 v4, v3, v4\n\t"
      "v_lshlrev_b32 v4, 4, v4\n\t"
      "v_add_u32 v30, v6, v4\n\t"
      "v_add_u32 v31, 0x2000, v30\n\t"
      "v_add_u32 v32, 0x4000, v30\n\t"
      "v_add_u32 v33, 0x6000, v30\n\t"
      "v_add_u32 v34, 0x8000, v30\n\t"
      "v_add_u32 v35, 0xa000, v30\n\t"
      "v_add_u32 v36, 0xc000, v30\n\t"
      "v_add_u32 v37, 0xe000, v30\n\t"
      "s_mul_i32 s93, s62, 458752\n\t"
      "s_lshl_b32 s59, s58, 7\n\t"
      "s_add_u32 s93, s93, s59\n\t"
      "s_lshl_b32 s71, s62, 13\n\t"
      "s_mov_b32 s42, 1835008\n\t"
      "s_mov_b32 s43, 0x20000\n\t"
      "s_mov_b32 s50, 16777216\n\t"
      "s_mov_b32 s51, 0x20000\n\t"
      "s_mov_b32 s40, s22\n\t"
      "s_and_b32 s41, s23, 0xffff\n\t"
      "s_mov_b32 s48, s26\n\t"
      "s_and_b32 s49, s27, 0xffff\n\t"
      "s_add_u32 m0, s70, 49152\n\t"
      "s_nop 0\n\t"
      "buffer_load_dwordx4 v28, s[84:87], s67 offen  lds\n\t"
      "s_add_u32 m0, s70, 57344\n\t"
      "s_nop 0\n\t"
      "buffer_load_dwordx4 v28, s[36:39], s67 offen  lds\n\t"
      "s_add_u32 m0, s70, 50176\n\t"
      "s_nop 0\n\t"
      "buffer_load_dwordx4 v29, s[84:87], s67 offen  lds\n\t"
      "s_add_u32 m0, s70, 58368\n\t"
      "s_nop 0\n\t"
      "buffer_load_dwordx4 v29, s[36:39], s67 offen  lds\n\t"
      "s_add_u32 m0, s69, 32768\n\t"
      "s_nop 0\n\t"
      "buffer_load_dwordx4 v24, s[28:31], s66 offen  lds\n\t"
      "s_add_u32 m0, s69, 33792\n\t"
      "s_nop 0\n\t"
      "buffer_load_dwordx4 v25, s[28:31], s66 offen  lds\n\t"
      "s_add_u32 m0, s69, 34816\n\t"
      "s_nop 0\n\t"
      "buffer_load_dwordx4 v26, s[28:31], s66 offen  lds\n\t"
      "s_add_u32 m0, s69, 35840\n\t"
      "s_nop 0\n\t"
      "buffer_load_dwordx4 v27, s[28:31], s66 offen  lds\n\t"
      "s_lshl_b32 s66, s63, 7\n\t"
      "s_add_u32 s66, s66, s79\n\t"
      "s_mul_i32 s67, s63, 229376\n\t"
      "s_add_u32 s67, s67, s92\n\t"
      "s_add_u32 s63, s63, 1\n\t"
      "s_and_b32 s63, s63, 7\n\t"
      "s_waitcnt vmcnt(8)\n\t"
      "s_barrier\n\t"
      "s_add_u32 m0, s70, 81920\n\t"
      "s_nop 0\n\t"
      "buffer_load_dwordx4 v28, s[84:87], s67 offen  lds\n\t"
      "s_add_u32 m0, s70, 90112\n\t"
      "s_nop 0\n\t"
      "buffer_load_dwordx4 v28, s[36:39], s67 offen  lds\n\t"
      "s_add_u32 m0, s70, 82944\n\t"
      "s_nop 0\n\t"
      "buffer_load_dwordx4 v29, s[84:87], s67 offen  lds\n\t"
      "s_add_u32 m0, s70, 91136\n\t"
      "s_nop 0\n\t"
      "buffer_load_dwordx4 v29, s[36:39], s67 offen  lds\n\t"
      "s_add_u32 m0, s69, 65536\n\t"
      "s_nop 0\n\t"
      "buffer_load_dwordx4 v24, s[28:31], s66 offen  lds\n\t"
      "s_add_u32 m0, s69, 66560\n\t"
      "s_nop 0\n\t"
      "buffer_load_dwordx4 v25, s[28:31], s66 offen  lds\n\t"
      "s_add_u32 m0, s69, 67584\n\t"
      "s_nop 0\n\t"
      "buffer_load_dwordx4 v26, s[28:31], s66 offen  lds\n\t"
      "s_add_u32 m0, s69, 68608\n\t"
      "s_nop 0\n\t"
      "buffer_load_dwordx4 v27, s[28:31], s66 offen  lds\n\t"
      "s_lshl_b32 s66, s63, 7\n\t"
      "s_add_u32 s66, s66, s79\n\t"
      "s_mul_i32 s67, s63, 229376\n\t"
      "s_add_u32 s67, s67, s92\n\t"
      "s_add_u32 s63, s63, 1\n\t"
      "s_and_b32 s63, s63, 7\n\t"
      "s_waitcnt vmcnt(8)\n\t"
      "s_barrier\n\t"
      "s_add_u32 m0, s70, 114688\n\t"
      "s_nop 0\n\t"
      "buffer_load_dwordx4 v28, s[84:87], s67 offen  lds\n\t"
      "s_add_u32 m0, s70, 122880\n\t"
      "s_nop 0\n\t"
      "buffer_load_dwordx4 v28, s[36:39], s67 offen  lds\n\t"
      "s_add_u32 m0, s70, 115712\n\t"
      "s_nop 0\n\t"
      "buffer_load_dwordx4 v29, s[84:87], s67 offen  lds\n\t"
      "s_add_u32 m0, s70, 123904\n\t"
      "s_nop 0\n\t"
      "buffer_load_dwordx4 v29, s[36:39], s67 offen  lds\n\t"
      "s_add_u32 m0, s69, 98304\n\t"
      "s_nop 0\n\t"
      "buffer_load_dwordx4 v24, s[28:31], s66 offen  lds\n\t"
      "s_add_u32 m0, s69, 99328\n\t"
      "s_nop 0\n\t"
      "buffer_load_dwordx4 v25, s[28:31], s66 offen  lds\n\t"
      "s_add_u32 m0, s69, 100352\n\t"
      "s_nop 0\n\t"
      "buffer_load_dwordx4 v26, s[28:31], s66 offen  lds\n\t"
      "s_add_u32 m0, s69, 101376\n\t"
      "s_nop 0\n\t"
      "buffer_load_dwordx4 v27, s[28:31], s66 offen  lds\n\t"
      "s_lshl_b32 s66, s63, 7\n\t"
      "s_add_u32 s66, s66, s79\n\t"
      "s_mul_i32 s67, s63, 229376\n\t"
      "s_add_u32 s67, s67, s92\n\t"
      "s_add_u32 s63, s63, 1\n\t"
      "s_and_b32 s63, s63, 7\n\t"
      "s_waitcnt vmcnt(8)\n\t"
      "s_barrier\n\t"
      "s_add_u32 m0, s70, 147456\n\t"
      "s_nop 0\n\t"
      "buffer_load_dwordx4 v28, s[84:87], s67 offen  lds\n\t"
      "s_add_u32 m0, s70, 155648\n\t"
      "s_nop 0\n\t"
      "buffer_load_dwordx4 v28, s[36:39], s67 offen  lds\n\t"
      "s_add_u32 m0, s70, 148480\n\t"
      "s_nop 0\n\t"
      "buffer_load_dwordx4 v29, s[84:87], s67 offen  lds\n\t"
      "s_add_u32 m0, s70, 156672\n\t"
      "s_nop 0\n\t"
      "buffer_load_dwordx4 v29, s[36:39], s67 offen  lds\n\t"
      "s_add_u32 m0, s69, 131072\n\t"
      "s_nop 0\n\t"
      "buffer_load_dwordx4 v24, s[28:31], s66 offen  lds\n\t"
      "s_add_u32 m0, s69, 132096\n\t"
      "s_nop 0\n\t"
      "buffer_load_dwordx4 v25, s[28:31], s66 offen  lds\n\t"
      "s_add_u32 m0, s69, 133120\n\t"
      "s_nop 0\n\t"
      "buffer_load_dwordx4 v26, s[28:31], s66 offen  lds\n\t"
      "s_add_u32 m0, s69, 134144\n\t"
      "s_nop 0\n\t"
      "buffer_load_dwordx4 v27, s[28:31], s66 offen  lds\n\t"
      "s_lshl_b32 s66, s63, 7\n\t"
      "s_add_u32 s66, s66, s79\n\t"
      "s_mul_i32 s67, s63, 229376\n\t"
      "s_add_u32 s67, s67, s92\n\t"
      "s_add_u32 s63, s63, 1\n\t"
      "s_and_b32 s63, s63, 7\n\t"
      "s_add_u32 m0, s70, 16384\n\t"
      "s_nop 0\n\t"
      "buffer_load_dwordx4 v28, s[84:87], s67 offen  lds\n\t"
      "s_add_u32 m0, s70, 24576\n\t"
      "s_nop 0\n\t"
      "buffer_load_dwordx4 v28, s[36:39], s67 offen  lds\n\t"
      "s_add_u32 m0, s70, 17408\n\t"
      "s_nop 0\n\t"
      "buffer_load_dwordx4 v29, s[84:87], s67 offen  lds\n\t"
      "s_add_u32 m0, s70, 25600\n\t"
      "s_nop 0\n\t"
      "buffer_load_dwordx4 v29, s[36:39], s67 offen  lds\n\t"
      "s_waitcnt vmcnt(12)\n\t"
      "s_barrier\n\t"
      "s_add_u32 m0, s69, 0\n\t"
      "s_nop 0\n\t"
      "buffer_load_dwordx4 v24, s[28:31], s66 offen  lds\n\t"
      "s_add_u32 m0, s69, 1024\n\t"
      "s_nop 0\n\t"
      "buffer_load_dwordx4 v25, s[28:31], s66 offen  lds\n\t"
      "s_add_u32 m0, s69, 2048\n\t"
      "s_nop 0\n\t"
      "buffer_load_dwordx4 v26, s[28:31], s66 offen  lds\n\t"
      "s_add_u32 m0, s69, 3072\n\t"
      "s_nop 0\n\t"
      "buffer_load_dwordx4 v27, s[28:31], s66 offen  lds\n\t"
      "s_lshl_b32 s66, s63, 7\n\t"
      "s_add_u32 s66, s66, s79\n\t"
      "s_mul_i32 s67, s63, 229376\n\t"
      "s_add_u32 s67, s67, s92\n\t"
      "s_add_u32 s63, s63, 1\n\t"
      "s_and_b32 s63, s63, 7\n\t"
      "s_add_u32 m0, s70, 49152\n\t"
      "s_nop 0\n\t"
      "buffer_load_dwordx4 v28, s[84:87], s67 offen  lds\n\t"
      "s_add_u32 m0, s70, 57344\n\t"
      "s_nop 0\n\t"
      "buffer_load_dwordx4 v28, s[36:39], s67 offen  lds\n\t"
      "s_add_u32 m0, s70, 50176\n\t"
      "s_nop 0\n\t"
      "buffer_load_dwordx4 v29, s[84:87], s67 offen  lds\n\t"
      "s_add_u32 m0, s70, 58368\n\t"
      "s_nop 0\n\t"
      "buffer_load_dwordx4 v29, s[36:39], s67 offen  lds\n\t"
      "s_waitcnt vmcnt(12)\n\t"
      "s_barrier\n\t"
      "s_add_u32 m0, s69, 32768\n\t"
      "s_nop 0\n\t"
      "buffer_load_dwordx4 v24, s[28:31], s66 offen  lds\n\t"
      "s_add_u32 m0, s69, 33792\n\t"
      "s_nop 0\n\t"
      "buffer_load_dwordx4 v25, s[28:31], s66 offen  lds\n\t"
      "s_add_u32 m0, s69, 34816\n\t"
      "s_nop 0\n\t"
      "buffer_load_dwordx4 v26, s[28:31], s66 offen  lds\n\t"
      "s_add_u32 m0, s69, 35840\n\t"
      "s_nop 0\n\t"
      "buffer_load_dwordx4 v27, s[28:31], s66 offen  lds\n\t"
      "s_lshl_b32 s66, s63, 7\n\t"
      "s_add_u32 s66, s66, s79\n\t"
      "s_mul_i32 s67, s63, 229376\n\t"
      "s_add_u32 s67, s67, s92\n\t"
      "s_add_u32 m0, s70, 81920\n\t"
      "s_nop 0\n\t"
      "buffer_load_dwordx4 v28, s[84:87], s67 offen  lds\n\t"
      "s_add_u32 m0, s70, 90112\n\t"
      "s_nop 0\n\t"
      "buffer_load_dwordx4 v28, s[36:39], s67 offen  lds\n\t"
      "s_add_u32 m0, s70, 82944\n\t"
      "s_nop 0\n\t"
      "buffer_load_dwordx4 v29, s[84:87], s67 offen  lds\n\t"
      "s_add_u32 m0, s70, 91136\n\t"
      "s_nop 0\n\t"
      "buffer_load_dwordx4 v29, s[36:39], s67 offen  lds\n\t"
      "s_waitcnt vmcnt(12)\n\t"
      "s_barrier\n\t"
      "s_add_u32 m0, s69, 65536\n\t"
      "s_nop 0\n\t"
      "buffer_load_dwordx4 v24, s[28:31], s66 offen  lds\n\t"
      "s_add_u32 m0, s69, 66560\n\t"
      "s_nop 0\n\t"
      "buffer_load_dwordx4 v25, s[28:31], s66 offen  lds\n\t"
      "s_add_u32 m0, s69, 67584\n\t"
      "s_nop 0\n\t"
      "buffer_load_dwordx4 v26, s[28:31], s66 offen  lds\n\t"
      "s_add_u32 m0, s69, 68608\n\t"
      "s_nop 0\n\t"
      "buffer_load_dwordx4 v27, s[28:31], s66 offen  lds\n\t"
      "s_lshl_b32 s68, s73, 16\n\t"
      "s_add_u32 s68, s68, s93\n\t"
      "s_add_u32 m0, s71, 98304\n\t"
      "s_nop 0\n\t"
      "buffer_load_dwordx4 v30, s[40:43], s68 offen  lds\n\t"
      "s_add_u32 m0, s71, 99328\n\t"
      "s_nop 0\n\t"
      "buffer_load_dwordx4 v31, s[40:43], s68 offen  lds\n\t"
      "s_add_u32 m0, s71, 100352\n\t"
      "s_nop 0\n\t"
      "buffer_load_dwordx4 v32, s[40:43], s68 offen  lds\n\t"
      "s_add_u32 m0, s71, 101376\n\t"
      "s_nop 0\n\t"
      "buffer_load_dwordx4 v33, s[40:43], s68 offen  lds\n\t"
      "s_waitcnt vmcnt(12)\n\t"
      "s_barrier\n\t"
      "s_add_u32 m0, s71, 102400\n\t"
      "s_nop 0\n\t"
      "buffer_load_dwordx4 v34, s[40:43], s68 offen  lds\n\t"
      "s_add_u32 m0, s71, 103424\n\t"
      "s_nop 0\n\t"
      "buffer_load_dwordx4 v35, s[40:43], s68 offen  lds\n\t"
      "s_add_u32 m0, s71, 104448\n\t"
      "s_nop 0\n\t"
      "buffer_load_dwordx4 v36, s[40:43], s68 offen  lds\n\t"
      "s_add_u32 m0, s71, 105472\n\t"
      "s_nop 0\n\t"
      "buffer_load_dwordx4 v37, s[40:43], s68 offen  lds\n\t"
      "s_add_u32 s73, s73, 1\n\t"
      "s_cmp_eq_u32 s73, 7\n\t"
      "s_cselect_b32 s73, 0, s73\n\t"
      "s_lshl_b32 s68, s73, 16\n\t"
      "s_add_u32 s68, s68, s93\n\t"
      "s_add_u32 m0, s71, 131072\n\t"
      "s_nop 0\n\t"
      "buffer_load_dwordx4 v30, s[40:43], s68 offen  lds\n\t"
      "s_add_u32 m0, s71, 132096\n\t"
      "s_nop 0\n\t"
      "buffer_load_dwordx4 v31, s[40:43], s68 offen  lds\n\t"
      "s_add_u32 m0, s71, 133120\n\t"
      "s_nop 0\n\t"
      "buffer_load_dwordx4 v32, s[40:43], s68 offen  lds\n\t"
      "s_add_u32 m0, s71, 134144\n\t"
      "s_nop 0\n\t"
      "buffer_load_dwordx4 v33, s[40:43], s68 offen  lds\n\t"
      "s_waitcnt vmcnt(12)\n\t"
      "s_barrier\n\t"
      "s_add_u32 m0, s71, 135168\n\t"
      "s_nop 0\n\t"
      "buffer_load_dwordx4 v34, s[40:43], s68 offen  lds\n\t"
      "s_add_u32 m0, s71, 136192\n\t"
      "s_nop 0\n\t"
      "buffer_load_dwordx4 v35, s[40:43], s68 offen  lds\n\t"
      "s_add_u32 m0, s71, 137216\n\t"
      "s_nop 0\n\t"
      "buffer_load_dwordx4 v36, s[40:43], s68 offen  lds\n\t"
      "s_add_u32 m0, s71, 138240\n\t"
      "s_nop 0\n\t"
      "buffer_load_dwordx4 v37, s[40:43], s68 offen  lds\n\t"
      "s_add_u32 s73, s73, 1\n\t"
      "s_cmp_eq_u32 s73, 7\n\t"
      "s_cselect_b32 s73, 0, s73\n\t"
      "s_lshl_b32 s68, s73, 16\n\t"
      "s_add_u32 s68, s68, s93\n\t"
      "s_add_u32 m0, s71, 0\n\t"
      "s_nop 0\n\t"
      "buffer_load_dwordx4 v30, s[40:43], s68 offen  lds\n\t"
      "s_add_u32 m0, s71, 1024\n\t"
      "s_nop 0\n\t"
      "buffer_load_dwordx4 v31, s[40:43], s68 offen  lds\n\t"
      "s_add_u32 m0, s71, 2048\n\t"
      "s_nop 0\n\t"
      "buffer_load_dwordx4 v32, s[40:43], s68 offen  lds\n\t"
      "s_add_u32 m0, s71, 3072\n\t"
      "s_nop 0\n\t"
      "buffer_load_dwordx4 v33, s[40:43], s68 offen  lds\n\t"
      "s_add_u32 m0, s71, 4096\n\t"
      "s_nop 0\n\t"
      "buffer_load_dwordx4 v34, s[40:43], s68 offen  lds\n\t"
      "s_add_u32 m0, s71, 5120\n\t"
      "s_nop 0\n\t"
      "buffer_load_dwordx4 v35, s[40:43], s68 offen  lds\n\t"
      "s_add_u32 m0, s71, 6144\n\t"
      "s_nop 0\n\t"
      "buffer_load_dwordx4 v36, s[40:43], s68 offen  lds\n\t"
      "s_add_u32 m0, s71, 7168\n\t"
      "s_nop 0\n\t"
      "buffer_load_dwordx4 v37, s[40:43], s68 offen  lds\n\t"
      "s_add_u32 s73, s73, 1\n\t"
      "s_cmp_eq_u32 s73, 7\n\t"
      "s_cselect_b32 s73, 0, s73\n\t"
      "s_waitcnt vmcnt(8)\n\t"
      "s_barrier\n\t"
      "s_lshl_b32 s68, s73, 16\n\t"
      "s_add_u32 s68, s68, s93\n\t"
      "s_add_u32 m0, s71, 32768\n\t"
      "s_nop 0\n\t"
      "buffer_load_dwordx4 v30, s[40:43], s68 offen  lds\n\t"
      "s_add_u32 m0, s71, 33792\n\t"
      "s_nop 0\n\t"
      "buffer_load_dwordx4 v31, s[40:43], s68 offen  lds\n\t"
      "s_add_u32 m0, s71, 34816\n\t"
      "s_nop 0\n\t"
      "buffer_load_dwordx4 v32, s[40:43], s68 offen  lds\n\t"
      "s_add_u32 m0, s71, 35840\n\t"
      "s_nop 0\n\t"
      "buffer_load_dwordx4 v33, s[40:43], s68 offen  lds\n\t"
      "s_add_u32 m0, s71, 36864\n\t"
      "s_nop 0\n\t"
      "buffer_load_dwordx4 v34, s[40:43], s68 offen  lds\n\t"
      "s_add_u32 m0, s71, 37888\n\t"
      "s_nop 0\n\t"
      "buffer_load_dwordx4 v35, s[40:43], s68 offen  lds\n\t"
      "s_add_u32 m0, s71, 38912\n\t"
      "s_nop 0\n\t"
      "buffer_load_dwordx4 v36, s[40:43], s68 offen  lds\n\t"
      "s_add_u32 m0, s71, 39936\n\t"
      "s_nop 0\n\t"
      "buffer_load_dwordx4 v37, s[40:43], s68 offen  lds\n\t"
      "s_add_u32 s73, s73, 1\n\t"
      "s_cmp_eq_u32 s73, 7\n\t"
      "s_cselect_b32 s73, 0, s73\n\t"
      "s_branch .Lld2%=\n\t"
      ".Lldnoprod%=:\n\t"
      "v_and_b32 v4, 6, v2\n\t"
      "v_xor_b32 v4, v3, v4\n\t"
      "v_lshlrev_b32 v4, 4, v4\n\t"
      "v_add_u32 v30, v6, v4\n\t"
      "v_add_u32 v31, 0x2000, v30\n\t"
      "v_add_u32 v32, 0x4000, v30\n\t"
      "v_add_u32 v33, 0x6000, v30\n\t"
      "v_add_u32 v34, 0x8000, v30\n\t"
      "v_add_u32 v35, 0xa000, v30\n\t"
      "v_add_u32 v36, 0xc000, v30\n\t"
      "v_add_u32 v37, 0xe000, v30\n\t"
      "s_mul_i32 s93, s62, 458752\n\t"
      "s_lshl_b32 s59, s58, 7\n\t"
      "s_add_u32 s93, s93, s59\n\t"
      "s_lshl_b32 s71, s62, 13\n\t"
      "s_mov_b32 s42, 1835008\n\t"
      "s_mov_b32 s43, 0x20000\n\t"
      "s_mov_b32 s50, 16777216\n\t"
      "s_mov_b32 s51, 0x20000\n\t"
      "s_mov_b32 s40, s22\n\t"
      "s_and_b32 s41, s23, 0xffff\n\t"
      "s_mov_b32 s48, s26\n\t"
      "s_and_b32 s49, s27, 0xffff\n\t"
      "s_and_b32 s59, s53, 1\n\t"
      "s_lshl_b32 s59, s59, 2\n\t"
      "s_lshr_b32 s60, s53, 1\n\t"
      "s_add_u32 s59, s59, s60\n\t"
      "s_lshl_b32 s59, s59, 2\n\t"
      "s_add_u32 s59, s59, s54\n\t"
      "s_sub_u32 s59, s59, 28\n\t"
      "s_lshl3_add_u32 s59, s59, s52\n\t"
      "s_lshl_b32 s59, s59, 12\n\t"
      "s_add_u32 s16, s16, s59\n\t"
      "s_addc_u32 s17, s17, 0\n\t"
      "v_lshrrev_b32 v49, 4, v1\n\t"
      "v_and_b32 v48, 15, v1\n\t"
      "v_lshlrev_b32 v48, 4, v48\n\t"
      "v_lshl_add_u32 v48, v49, 10, v48\n\t"
      "s_bitcmp1_b32 s53, 0\n\t"
      "s_cbranch_scc1 .Lxp522_%=\n\t"
      "global_load_dwordx4 v[52:55], v48, s[16:17] offset:0\n\t"
      "global_load_dwordx4 v[56:59], v48, s[16:17] offset:256\n\t"
      "global_load_dwordx4 v[60:63], v48, s[16:17] offset:512\n\t"
      "global_load_dwordx4 v[64:67], v48, s[16:17] offset:768\n\t"
      "v_add_u32 v50, 0x80000, v48\n\t"
      "global_load_dwordx4 v[52:55], v50, s[16:17] offset:0\n\t"
      "global_load_dwordx4 v[56:59], v50, s[16:17] offset:256\n\t"
      "global_load_dwordx4 v[60:63], v50, s[16:17] offset:512\n\t"
      "global_load_dwordx4 v[64:67], v50, s[16:17] offset:768\n\t"
      ".Lxp522_%=:\n\t"
      "s_lshl_b32 s68, s73, 16\n\t"
      "s_add_u32 s68, s68, s93\n\t"
      "s_add_u32 m0, s71, 98304\n\t"
      "s_nop 0\n\t"
      "buffer_load_dwordx4 v30, s[40:43], s68 offen  lds\n\t"
      "s_add_u32 m0, s71, 99328\n\t"
      "s_nop 0\n\t"
      "buffer_load_dwordx4 v31, s[40:43], s68 offen  lds\n\t"
      "s_add_u32 m0, s71, 100352\n\t"
      "s_nop 0\n\t"
      "buffer_load_dwordx4 v32, s[40:43], s68 offen  lds\n\t"
      "s_add_u32 m0, s71, 101376\n\t"
      "s_nop 0\n\t"
      "buffer_load_dwordx4 v33, s[40:43], s68 offen  lds\n\t"
      "s_add_u32 m0, s71, 102400\n\t"
      "s_nop 0\n\t"
      "buffer_load_dwordx4 v34, s[40:43], s68 offen  lds\n\t"
      "s_add_u32 m0, s71, 103424\n\t"
      "s_nop 0\n\t"
      "buffer_load_dwordx4 v35, s[40:43], s68 offen  lds\n\t"
      "s_add_u32 m0, s71, 104448\n\t"
      "s_nop 0\n\t"
      "buffer_load_dwordx4 v36, s[40:43], s68 offen  lds\n\t"
      "s_add_u32 m0, s71, 105472\n\t"
      "s_nop 0\n\t"
      "buffer_load_dwordx4 v37, s[40:43], s68 offen  lds\n\t"
      "s_add_u32 s73, s73, 1\n\t"
      "s_cmp_eq_u32 s73, 7\n\t"
      "s_cselect_b32 s73, 0, s73\n\t"
      "s_lshl_b32 s68, s73, 16\n\t"
      "s_add_u32 s68, s68, s93\n\t"
      "s_add_u32 m0, s71, 131072\n\t"
      "s_nop 0\n\t"
      "buffer_load_dwordx4 v30, s[40:43], s68 offen  lds\n\t"
      "s_add_u32 m0, s71, 132096\n\t"
      "s_nop 0\n\t"
      "buffer_load_dwordx4 v31, s[40:43], s68 offen  lds\n\t"
      "s_add_u32 m0, s71, 133120\n\t"
      "s_nop 0\n\t"
      "buffer_load_dwordx4 v32, s[40:43], s68 offen  lds\n\t"
      "s_add_u32 m0, s71, 134144\n\t"
      "s_nop 0\n\t"
      "buffer_load_dwordx4 v33, s[40:43], s68 offen  lds\n\t"
      "s_add_u32 m0, s71, 135168\n\t"
      "s_nop 0\n\t"
      "buffer_load_dwordx4 v34, s[40:43], s68 offen  lds\n\t"
      "s_add_u32 m0, s71, 136192\n\t"
      "s_nop 0\n\t"
      "buffer_load_dwordx4 v35, s[40:43], s68 offen  lds\n\t"
      "s_add_u32 m0, s71, 137216\n\t"
      "s_nop 0\n\t"
      "buffer_load_dwordx4 v36, s[40:43], s68 offen  lds\n\t"
      "s_add_u32 m0, s71, 138240\n\t"
      "s_nop 0\n\t"
      "buffer_load_dwordx4 v37, s[40:43], s68 offen  lds\n\t"
      "s_add_u32 s73, s73, 1\n\t"
      "s_cmp_eq_u32 s73, 7\n\t"
      "s_cselect_b32 s73, 0, s73\n\t"
      "s_lshl_b32 s68, s73, 16\n\t"
      "s_add_u32 s68, s68, s93\n\t"
      "s_add_u32 m0, s71, 0\n\t"
      "s_nop 0\n\t"
      "buffer_load_dwordx4 v30, s[40:43], s68 offen  lds\n\t"
      "s_add_u32 m0, s71, 1024\n\t"
      "s_nop 0\n\t"
      "buffer_load_dwordx4 v31, s[40:43], s68 offen  lds\n\t"
      "s_add_u32 m0, s71, 2048\n\t"
      "s_nop 0\n\t"
      "buffer_load_dwordx4 v32, s[40:43], s68 offen  lds\n\t"
      "s_add_u32 m0, s71, 3072\n\t"
      "s_nop 0\n\t"
      "buffer_load_dwordx4 v33, s[40:43], s68 offen  lds\n\t"
      "s_add_u32 m0, s71, 4096\n\t"
      "s_nop 0\n\t"
      "buffer_load_dwordx4 v34, s[40:43], s68 offen  lds\n\t"
      "s_add_u32 m0, s71, 5120\n\t"
      "s_nop 0\n\t"
      "buffer_load_dwordx4 v35, s[40:43], s68 offen  lds\n\t"
      "s_add_u32 m0, s71, 6144\n\t"
      "s_nop 0\n\t"
      "buffer_load_dwordx4 v36, s[40:43], s68 offen  lds\n\t"
      "s_add_u32 m0, s71, 7168\n\t"
      "s_nop 0\n\t"
      "buffer_load_dwordx4 v37, s[40:43], s68 offen  lds\n\t"
      "s_add_u32 s73, s73, 1\n\t"
      "s_cmp_eq_u32 s73, 7\n\t"
      "s_cselect_b32 s73, 0, s73\n\t"
      "s_lshl_b32 s68, s73, 16\n\t"
      "s_add_u32 s68, s68, s93\n\t"
      "s_add_u32 m0, s71, 32768\n\t"
      "s_nop 0\n\t"
      "buffer_load_dwordx4 v30, s[40:43], s68 offen  lds\n\t"
      "s_add_u32 m0, s71, 33792\n\t"
      "s_nop 0\n\t"
      "buffer_load_dwordx4 v31, s[40:43], s68 offen  lds\n\t"
      "s_add_u32 m0, s71, 34816\n\t"
      "s_nop 0\n\t"
      "buffer_load_dwordx4 v32, s[40:43], s68 offen  lds\n\t"
      "s_add_u32 m0, s71, 35840\n\t"
      "s_nop 0\n\t"
      "buffer_load_dwordx4 v33, s[40:43], s68 offen  lds\n\t"
      "s_add_u32 m0, s71, 36864\n\t"
      "s_nop 0\n\t"
      "buffer_load_dwordx4 v34, s[40:43], s68 offen  lds\n\t"
      "s_add_u32 m0, s71, 37888\n\t"
      "s_nop 0\n\t"
      "buffer_load_dwordx4 v35, s[40:43], s68 offen  lds\n\t"
      "s_add_u32 m0, s71, 38912\n\t"
      "s_nop 0\n\t"
      "buffer_load_dwordx4 v36, s[40:43], s68 offen  lds\n\t"
      "s_add_u32 m0, s71, 39936\n\t"
      "s_nop 0\n\t"
      "buffer_load_dwordx4 v37, s[40:43], s68 offen  lds\n\t"
      "s_add_u32 s73, s73, 1\n\t"
      "s_cmp_eq_u32 s73, 7\n\t"
      "s_cselect_b32 s73, 0, s73\n\t"
      "s_lshl_b32 s68, s73, 16\n\t"
      "s_add_u32 s68, s68, s93\n\t"
      "s_add_u32 m0, s71, 65536\n\t"
      "s_nop 0\n\t"
      "buffer_load_dwordx4 v30, s[40:43], s68 offen  lds\n\t"
      "s_add_u32 m0, s71, 66560\n\t"
      "s_nop 0\n\t"
      "buffer_load_dwordx4 v31, s[40:43], s68 offen  lds\n\t"
      "s_add_u32 m0, s71, 67584\n\t"
      "s_nop 0\n\t"
      "buffer_load_dwordx4 v32, s[40:43], s68 offen  lds\n\t"
      "s_add_u32 m0, s71, 68608\n\t"
      "s_nop 0\n\t"
      "buffer_load_dwordx4 v33, s[40:43], s68 offen  lds\n\t"
      "s_add_u32 m0, s71, 69632\n\t"
      "s_nop 0\n\t"
      "buffer_load_dwordx4 v34, s[40:43], s68 offen  lds\n\t"
      "s_add_u32 m0, s71, 70656\n\t"
      "s_nop 0\n\t"
      "buffer_load_dwordx4 v35, s[40:43], s68 offen  lds\n\t"
      "s_add_u32 m0, s71, 71680\n\t"
      "s_nop 0\n\t"
      "buffer_load_dwordx4 v36, s[40:43], s68 offen  lds\n\t"
      "s_add_u32 m0, s71, 72704\n\t"
      "s_nop 0\n\t"
      "buffer_load_dwordx4 v37, s[40:43], s68 offen  lds\n\t"
      "s_add_u32 s73, s73, 1\n\t"
      "s_cmp_eq_u32 s73, 7\n\t"
      "s_cselect_b32 s73, 0, s73\n\t"
      "s_waitcnt vmcnt(24)\n\t"
      "s_barrier\n\t"
      ".Lld2%=:\n\t"
      "s_barrier\n\t"
      "s_cmp_ge_u32 s54, 28\n\t"
      "s_cbranch_scc1 .Lnod4%=\n\t"
      "s_lshl_b32 s68, s73, 16\n\t"
      "s_add_u32 s68, s68, s93\n\t"
      "s_add_u32 m0, s71, 65536\n\t"
      "s_nop 0\n\t"
      "buffer_load_dwordx4 v30, s[40:43], s68 offen  lds\n\t"
      "s_add_u32 m0, s71, 66560\n\t"
      "s_nop 0\n\t"
      "buffer_load_dwordx4 v31, s[40:43], s68 offen  lds\n\t"
      "s_add_u32 m0, s71, 67584\n\t"
      "s_nop 0\n\t"
      "buffer_load_dwordx4 v32, s[40:43], s68 offen  lds\n\t"
      "s_add_u32 m0, s71, 68608\n\t"
      "s_nop 0\n\t"
      "buffer_load_dwordx4 v33, s[40:43], s68 offen  lds\n\t"
      "s_add_u32 m0, s71, 69632\n\t"
      "s_nop 0\n\t"
      "buffer_load_dwordx4 v34, s[40:43], s68 offen  lds\n\t"
      "s_add_u32 m0, s71, 70656\n\t"
      "s_nop 0\n\t"
      "buffer_load_dwordx4 v35, s[40:43], s68 offen  lds\n\t"
      "s_add_u32 m0, s71, 71680\n\t"
      "s_nop 0\n\t"
      "buffer_load_dwordx4 v36, s[40:43], s68 offen  lds\n\t"
      "s_add_u32 m0, s71, 72704\n\t"
      "s_nop 0\n\t"
      "buffer_load_dwordx4 v37, s[40:43], s68 offen  lds\n\t"
      "s_add_u32 s73, s73, 1\n\t"
      "s_cmp_eq_u32 s73, 7\n\t"
      "s_cselect_b32 s73, 0, s73\n\t"
      ".Lnod4%=:\n\t"
      "s_lshl_b32 s68, s73, 16\n\t"
      "s_add_u32 s68, s68, s93\n\t"
      "s_add_u32 m0, s71, 98304\n\t"
      "s_nop 0\n\t"
      "buffer_load_dwordx4 v30, s[40:43], s68 offen  lds\n\t"
      "s_add_u32 m0, s71, 99328\n\t"
      "s_nop 0\n\t"
      "buffer_load_dwordx4 v31, s[40:43], s68 offen  lds\n\t"
      "s_add_u32 m0, s71, 100352\n\t"
      "s_nop 0\n\t"
      "buffer_load_dwordx4 v32, s[40:43], s68 offen  lds\n\t"
      "s_add_u32 m0, s71, 101376\n\t"
      "s_nop 0\n\t"
      "buffer_load_dwordx4 v33, s[40:43], s68 offen  lds\n\t"
      "s_add_u32 m0, s71, 102400\n\t"
      "s_nop 0\n\t"
      "buffer_load_dwordx4 v34, s[40:43], s68 offen  lds\n\t"
      "s_add_u32 m0, s71, 103424\n\t"
      "s_nop 0\n\t"
      "buffer_load_dwordx4 v35, s[40:43], s68 offen  lds\n\t"
      "s_add_u32 m0, s71, 104448\n\t"
      "s_nop 0\n\t"
      "buffer_load_dwordx4 v36, s[40:43], s68 offen  lds\n\t"
      "s_add_u32 m0, s71, 105472\n\t"
      "s_nop 0\n\t"
      "buffer_load_dwordx4 v37, s[40:43], s68 offen  lds\n\t"
      "s_add_u32 s73, s73, 1\n\t"
      "s_cmp_eq_u32 s73, 7\n\t"
      "s_cselect_b32 s73, 0, s73\n\t"
      "s_barrier\n\t"
      "s_lshl_b32 s68, s73, 16\n\t"
      "s_add_u32 s68, s68, s93\n\t"
      "s_add_u32 m0, s71, 131072\n\t"
      "s_nop 0\n\t"
      "buffer_load_dwordx4 v30, s[40:43], s68 offen  lds\n\t"
      "s_add_u32 m0, s71, 132096\n\t"
      "s_nop 0\n\t"
      "buffer_load_dwordx4 v31, s[40:43], s68 offen  lds\n\t"
      "s_add_u32 m0, s71, 133120\n\t"
      "s_nop 0\n\t"
      "buffer_load_dwordx4 v32, s[40:43], s68 offen  lds\n\t"
      "s_add_u32 m0, s71, 134144\n\t"
      "s_nop 0\n\t"
      "buffer_load_dwordx4 v33, s[40:43], s68 offen  lds\n\t"
      "s_add_u32 m0, s71, 135168\n\t"
      "s_nop 0\n\t"
      "buffer_load_dwordx4 v34, s[40:43], s68 offen  lds\n\t"
      "s_add_u32 m0, s71, 136192\n\t"
      "s_nop 0\n\t"
      "buffer_load_dwordx4 v35, s[40:43], s68 offen  lds\n\t"
      "s_add_u32 m0, s71, 137216\n\t"
      "s_nop 0\n\t"
      "buffer_load_dwordx4 v36, s[40:43], s68 offen  lds\n\t"
      "s_add_u32 m0, s71, 138240\n\t"
      "s_nop 0\n\t"
      "buffer_load_dwordx4 v37, s[40:43], s68 offen  lds\n\t"
      "s_add_u32 s73, s73, 1\n\t"
      "s_cmp_eq_u32 s73, 7\n\t"
      "s_cselect_b32 s73, 0, s73\n\t"
      "s_cmp_lg_u32 %1, 255\n\t"
      "s_cbranch_scc1 .Lnobump%=\n\t"
      "s_cmp_lg_u32 s52, 4\n\t"
      "s_cbranch_scc1 .Lnobump%=\n\t"
      "v_mov_b32 v7, 4325376\n\t"
      "buffer_load_dwordx2 v[18:19], v7, s[48:51], 0 offen sc1\n\t"
      "v_lshlrev_b32 v8, 9, v1\n\t"
      "v_add_u32 v8, 4259840, v8\n\t"
      "s_waitcnt vmcnt(0)\n\t"
      "v_readfirstlane_b32 s64, v18\n\t"
      "v_readfirstlane_b32 s65, v19\n\t"
      "s_nop 3\n\t"
      "s_add_u32 s64, s64, 1\n\t"
      "s_addc_u32 s65, s65, 0\n\t"
      "v_mov_b32 v18, s64\n\t"
      "v_mov_b32 v19, s65\n\t"
      "s_mov_b32 s72, 0\n\t"
      ".Lbump%=:\n\t"
      "buffer_load_dwordx2 v[40:41], v8, s[48:51], 0 offen offset:0 sc1\n\t"
      "buffer_load_dwordx2 v[42:43], v8, s[48:51], 0 offen offset:128 sc1\n\t"
      "buffer_load_dwordx2 v[44:45], v8, s[48:51], 0 offen offset:256 sc1\n\t"
      "buffer_load_dwordx2 v[46:47], v8, s[48:51], 0 offen offset:384 sc1\n\t"
      "s_waitcnt vmcnt(0)\n\t"
      "v_cmp_eq_u64 vcc, s[64:65], v[40:41]\n\t"
      "v_cmp_eq_u64 s[74:75], s[64:65], v[42:43]\n\t"
      "s_nop 3\n\t"
      "s_and_b64 vcc, vcc, s[74:75]\n\t"
      "v_cmp_eq_u64 s[74:75], s[64:65], v[44:45]\n\t"
      "s_nop 3\n\t"
      "s_and_b64 vcc, vcc, s[74:75]\n\t"
      "v_cmp_eq_u64 s[74:75], s[64:65], v[46:47]\n\t"
      "s_nop 3\n\t"
      "s_and_b64 vcc, vcc, s[74:75]\n\t"
      "s_cmp_eq_u64 vcc, exec\n\t"
      "s_cbranch_scc1 .Lbumpgo%=\n\t"
      "s_sleep 2\n\t"
      "s_add_u32 s72, s72, 1\n\t"
      "s_cmp_lt_u32 s72, 0x8000\n\t"
      "s_cbranch_scc1 .Lbump%=\n\t"
      ".Lbumpgo%=:\n\t"
      "s_mov_b64 exec, 1\n\t"
      "buffer_store_dwordx2 v[18:19], v7, s[48:51], 0 offen sc1\n\t"
      "s_mov_b64 exec, -1\n\t"
      ".Lnobump%=:\n\t"
      "s_waitcnt vmcnt(16)\n\t"
      "s_barrier\n\t"
      "s_waitcnt vmcnt(0)\n\t"
      "s_barrier\n\t"
      "s_endpgm\n\t"
      ".fill 458, 4, 0xBF800000\n\t"
      ".Lcompute%=:\n\t"
      "v_lshrrev_b32 v2, 5, v1\n\t"
      "v_and_b32 v3, 31, v1\n\t"
      "v_bfe_u32 v4, v1, 2, 2\n\t"
      "v_and_b32 v5, 3, v1\n\t"
      "v_bfe_u32 v6, v1, 4, 1\n\t"
      "v_lshlrev_b32 v16, 4, v1\n\t"
      "s_mov_b32 s46, 1048576\n\t"
      "s_mov_b32 s47, 0x20000\n\t"
      "s_mov_b32 s50, 16777216\n\t"
      "s_mov_b32 s51, 0x20000\n\t"
      "v_mov_b32 v7, 4325376\n\t"
      "s_waitcnt lgkmcnt(0)\n\t"
      "s_mov_b32 s48, s26\n\t"
      "s_and_b32 s49, s27, 0xffff\n\t"
      "s_mov_b32 s44, s24\n\t"
      "s_and_b32 s45, s25, 0xffff\n\t"
      "s_cmp_lg_u32 s52, 1\n\t"
      "s_cbranch_scc1 .Lnotlb%=\n\t"
      "s_lshl_b32 s59, %1, 7\n\t"
      "s_add_u32 s60, s59, 3932160\n\t"
      "s_lshl_b32 s61, s57, 15\n\t"
      "s_lshl_b32 s62, s58, 7\n\t"
      "s_add_u32 s61, s61, s62\n\t"
      "v_mov_b32 v21, 0\n\t"
      "s_mov_b64 exec, 1\n\t"
      "buffer_load_dword v20, v21, s[48:51], s59 offen\n\t"
      "buffer_load_dword v20, v21, s[48:51], s60 offen\n\t"
      "buffer_load_dword v20, v21, s[44:47], s61 offen\n\t"
      "v_mov_b32 v21, s62\n\t"
      "global_load_dword v20, v21, s[22:23]\n\t"
      "s_mov_b64 exec, -1\n\t"
      ".Lnotlb%=:\n\t"
      "s_mov_b64 exec, 1\n\t"
      "buffer_load_dwordx2 v[18:19], v7, s[48:51], 0 offen sc1\n\t"
      "s_mov_b64 exec, -1\n\t"
      "s_lshr_b32 s59, s52, 1\n\t"
      "s_and_b32 s60, s52, 1\n\t"
      "v_lshrrev_b32 v2, 4, v1\n\t"
      "v_and_b32 v3, 15, v1\n\t"
      "s_lshl_b32 s61, s59, 13\n\t"
      "v_lshlrev_b32 v7, 7, v3\n\t"
      "v_add_u32 v7, s61, v7\n\t"
      "v_lshrrev_b32 v8, 1, v3\n\t"
      "v_xor_b32 v8, v8, v2\n\t"
      "v_xor_b32 v9, 0, v8\n\t"
      "v_lshl_add_u32 v152, v9, 4, v7\n\t"
      "v_add_u32 v156, 0x10000, v152\n\t"
      "v_add_u32 v160, 0x20000, v152\n\t"
      "v_xor_b32 v9, 4, v8\n\t"
      "v_lshl_add_u32 v153, v9, 4, v7\n\t"
      "v_add_u32 v157, 0x10000, v153\n\t"
      "v_add_u32 v161, 0x20000, v153\n\t"
      "v_lshrrev_b32 v8, 1, v4\n\t"
      "v_lshl_or_b32 v8, v6, 1, v8\n\t"
      "v_lshlrev_b32 v7, 3, v5\n\t"
      "v_lshl_add_u32 v9, v2, 3, v4\n\t"
      "v_lshl_add_u32 v9, v9, 7, v7\n\t"
      "s_lshl_b32 s61, s60, 1\n\t"
      "v_xor_b32 v165, s61, v8\n\t"
      "v_lshl_add_u32 v9, v165, 5, v9\n\t"
      "v_add_u32 v164, 0x4000, v9\n\t"
      "v_xor_b32 v165, 32, v164\n\t"
      "v_add_u32 v166, 0x14000, v9\n\t"
      "v_xor_b32 v167, 32, v166\n\t"
      "v_add_u32 v168, 0x24000, v9\n\t"
      "v_xor_b32 v169, 32, v168\n\t"
      "s_lshl_b32 s61, s52, 13\n\t"
      "s_add_u32 s61, s61, 0x10000\n\t"
      "v_lshl_add_u32 v9, v2, 2, v4\n\t"
      "v_lshl_add_u32 v9, v9, 7, v7\n\t"
      "v_add_u32 v9, s61, v9\n\t"
      "v_lshl_add_u32 v10, v8, 5, v9\n\t"
      "v_xor_b32 v11, 32, v10\n\t"
      "v_xor_b32 v12, 64, v10\n\t"
      "v_xor_b32 v13, 96, v10\n\t"
      "s_lshr_b32 s61, s57, 2\n\t"
      "s_and_b32 s62, s61, 3\n\t"
      "s_lshl_b32 s62, s62, 3\n\t"
      "s_lshr_b32 s61, s61, 2\n\t"
      "s_add_u32 s62, s62, s61\n\t"
      "s_lshl1_add_u32 s62, s52, s62\n\t"
      "s_lshl_b32 s62, s62, 8\n\t"
      "s_bfe_u32 s61, s57, 0x10001\n\t"
      "s_lshl_b32 s61, s61, 7\n\t"
      "s_add_u32 s62, s62, s61\n\t"
      "s_add_u32 s62, s62, 4194304\n\t"
      "v_lshrrev_b32 v17, 1, v1\n\t"
      "v_lshlrev_b32 v17, 13, v17\n\t"
      "v_and_b32 v9, 1, v1\n\t"
      "v_lshl_add_u32 v17, v9, 6, v17\n\t"
      "v_add_u32 v17, s62, v17\n\t"
      "s_mov_b32 s76, 0xbfb8aa3b\n\t"
      "s_mov_b32 s77, 0xbfb8aa3b\n\t"
      "s_lshl1_add_u32 s61, s55, s59\n\t"
      "s_mul_i32 s61, s61, 112\n\t"
      "s_lshl1_add_u32 s62, s56, s60\n\t"
      "s_add_u32 s61, s62, s61\n\t"
      "s_lshl_b32 s78, s61, 11\n\t"
      "s_add_u32 s78, s78, 65536\n\t"
      "s_add_u32 s66, s78, 114688\n\t"
      "s_lshl3_add_u32 s67, s54, s53\n\t"
      "s_lshl_b32 s67, s67, 8\n\t"
      "s_lshl_b32 s59, s59, 7\n\t"
      "s_lshl_b32 s61, s60, 6\n\t"
      "s_add_u32 s59, s59, s61\n\t"
      "s_add_u32 s67, s67, s59\n\t"
      "s_mul_i32 s93, s57, 112\n\t"
      "s_mul_i32 s59, s52, 28\n\t"
      "s_add_u32 s93, s93, s59\n\t"
      "s_lshl_b32 s93, s93, 10\n\t"
      "s_add_u32 s93, s93, 65536\n\t"
      "s_cmp_ge_u32 s54, 28\n\t"
      "s_cbranch_scc1 .Lnoprod%=\n\t"
      "s_barrier\n\t"
      "ds_read_b128 v[88:91], v152 offset:0\n\t"
      "ds_read_b64_tr_b16 v[104:105], v164 offset:0\n\t"
      "ds_read_b64_tr_b16 v[106:107], v164 offset:512\n\t"
      "ds_read_b128 v[92:95], v152 offset:2048\n\t"
      "ds_read_b128 v[96:99], v152 offset:4096\n\t"
      "ds_read_b128 v[100:103], v152 offset:6144\n\t"
      "ds_read_b64_tr_b16 v[108:109], v165 offset:0\n\t"
      "ds_read_b64_tr_b16 v[110:111], v165 offset:512\n\t"
      "ds_read_b64_tr_b16 v[112:113], v164 offset:8192\n\t"
      "ds_read_b64_tr_b16 v[114:115], v164 offset:8704\n\t"
      "ds_read_b64_tr_b16 v[116:117], v165 offset:8192\n\t"
      "ds_read_b64_tr_b16 v[118:119], v165 offset:8704\n\t"
      "s_waitcnt lgkmcnt(6)\n\t"
      "v_mfma_f32_16x16x32_bf16 v[24:27], v[104:107], v[88:91], 0\n\t"
      "ds_read_b128 v[120:123], v153 offset:0\n\t"
      "v_mfma_f32_16x16x32_bf16 v[32:35], v[104:107], v[92:95], 0\n\t"
      "ds_read_b64_tr_b16 v[136:137], v164 offset:4096\n\t"
      "v_mfma_f32_16x16x32_bf16 v[40:43], v[104:107], v[96:99], 0\n\t"
      "ds_read_b64_tr_b16 v[138:139], v164 offset:4608\n\t"
      "v_mfma_f32_16x16x32_bf16 v[48:51], v[104:107], v[100:103], 0\n\t"
      "ds_read_b128 v[124:127], v153 offset:2048\n\t"
      "s_waitcnt lgkmcnt(8)\n\t"
      "v_mfma_f32_16x16x32_bf16 v[28:31], v[108:111], v[88:91], 0\n\t"
      "ds_read_b128 v[128:131], v153 offset:4096\n\t"
      "v_mfma_f32_16x16x32_bf16 v[36:39], v[108:111], v[92:95], 0\n\t"
      "ds_read_b128 v[132:135], v153 offset:6144\n\t"
      "v_mfma_f32_16x16x32_bf16 v[44:47], v[108:111], v[96:99], 0\n\t"
      "ds_read_b64_tr_b16 v[140:141], v165 offset:4096\n\t"
      "v_mfma_f32_16x16x32_bf16 v[52:55], v[108:111], v[100:103], 0\n\t"
      "ds_read_b64_tr_b16 v[142:143], v165 offset:4608\n\t"
      "s_waitcnt lgkmcnt(10)\n\t"
      "v_mfma_f32_16x16x32_bf16 v[56:59], v[112:115], v[88:91], 0\n\t"
      "ds_read_b64_tr_b16 v[144:145], v164 offset:12288\n\t"
      "v_mfma_f32_16x16x32_bf16 v[64:67], v[112:115], v[92:95], 0\n\t"
      "ds_read_b64_tr_b16 v[146:147], v164 offset:12800\n\t"
      "v_mfma_f32_16x16x32_bf16 v[72:75], v[112:115], v[96:99], 0\n\t"
      "ds_read_b64_tr_b16 v[148:149], v165 offset:12288\n\t"
      "v_mfma_f32_16x16x32_bf16 v[80:83], v[112:115], v[100:103], 0\n\t"
      "ds_read_b64_tr_b16 v[150:151], v165 offset:12800\n\t"
      "s_waitcnt lgkmcnt(12)\n\t"
      "v_mfma_f32_16x16x32_bf16 v[60:63], v[116:119], v[88:91], 0\n\t"
      "v_mfma_f32_16x16x32_bf16 v[68:71], v[116:119], v[92:95], 0\n\t"
      "v_mfma_f32_16x16x32_bf16 v[76:79], v[116:119], v[96:99], 0\n\t"
      "v_mfma_f32_16x16x32_bf16 v[84:87], v[116:119], v[100:103], 0\n\t"
      "s_waitcnt lgkmcnt(6)\n\t"
      "v_mfma_f32_16x16x32_bf16 v[24:27], v[136:139], v[120:123], v[24:27]\n\t"
      "s_barrier\n\t"
      "ds_read_b128 v[88:91], v152 offset:32768\n\t"
      "v_mfma_f32_16x16x32_bf16 v[32:35], v[136:139], v[124:127], v[32:35]\n\t"
      "ds_read_b64_tr_b16 v[104:105], v164 offset:32768\n\t"
      "v_mfma_f32_16x16x32_bf16 v[40:43], v[136:139], v[128:131], v[40:43]\n\t"
      "ds_read_b64_tr_b16 v[106:107], v164 offset:33280\n\t"
      "v_mfma_f32_16x16x32_bf16 v[48:51], v[136:139], v[132:135], v[48:51]\n\t"
      "ds_read_b128 v[92:95], v152 offset:34816\n\t"
      "s_waitcnt lgkmcnt(8)\n\t"
      "v_mfma_f32_16x16x32_bf16 v[28:31], v[140:143], v[120:123], v[28:31]\n\t"
      "ds_read_b128 v[96:99], v152 offset:36864\n\t"
      "v_mfma_f32_16x16x32_bf16 v[36:39], v[140:143], v[124:127], v[36:39]\n\t"
      "ds_read_b128 v[100:103], v152 offset:38912\n\t"
      "v_mfma_f32_16x16x32_bf16 v[44:47], v[140:143], v[128:131], v[44:47]\n\t"
      "ds_read_b64_tr_b16 v[108:109], v165 offset:32768\n\t"
      "v_mfma_f32_16x16x32_bf16 v[52:55], v[140:143], v[132:135], v[52:55]\n\t"
      "ds_read_b64_tr_b16 v[110:111], v165 offset:33280\n\t"
      "s_waitcnt lgkmcnt(10)\n\t"
      "v_mfma_f32_16x16x32_bf16 v[56:59], v[144:147], v[120:123], v[56:59]\n\t"
      "ds_read_b64_tr_b16 v[112:113], v164 offset:40960\n\t"
      "v_mfma_f32_16x16x32_bf16 v[64:67], v[144:147], v[124:127], v[64:67]\n\t"
      "ds_read_b64_tr_b16 v[114:115], v164 offset:41472\n\t"
      "v_mfma_f32_16x16x32_bf16 v[72:75], v[144:147], v[128:131], v[72:75]\n\t"
      "ds_read_b64_tr_b16 v[116:117], v165 offset:40960\n\t"
      "v_mfma_f32_16x16x32_bf16 v[80:83], v[144:147], v[132:135], v[80:83]\n\t"
      "ds_read_b64_tr_b16 v[118:119], v165 offset:41472\n\t"
      "s_waitcnt lgkmcnt(12)\n\t"
      "v_mfma_f32_16x16x32_bf16 v[60:63], v[148:151], v[120:123], v[60:63]\n\t"
      "s_waitcnt vmcnt(0)\n\t"
      "v_readfirstlane_b32 s64, v18\n\t"
      "v_mfma_f32_16x16x32_bf16 v[68:71], v[148:151], v[124:127], v[68:71]\n\t"
      "v_readfirstlane_b32 s65, v19\n\t"
      "s_nop 3\n\t"
      "v_mfma_f32_16x16x32_bf16 v[76:79], v[148:151], v[128:131], v[76:79]\n\t"
      "s_add_u32 s64, s64, 1\n\t"
      "s_addc_u32 s65, s65, 0\n\t"
      "v_mfma_f32_16x16x32_bf16 v[84:87], v[148:151], v[132:135], v[84:87]\n\t"
      "v_mov_b32 v18, s64\n\t"
      "v_mov_b32 v19, s65\n\t"
      "s_waitcnt lgkmcnt(6)\n\t"
      "v_mfma_f32_16x16x32_bf16 v[24:27], v[104:107], v[88:91], v[24:27]\n\t"
      "ds_read_b128 v[120:123], v153 offset:32768\n\t"
      "v_mfma_f32_16x16x32_bf16 v[32:35], v[104:107], v[92:95], v[32:35]\n\t"
      "ds_read_b64_tr_b16 v[136:137], v164 offset:36864\n\t"
      "v_mfma_f32_16x16x32_bf16 v[40:43], v[104:107], v[96:99], v[40:43]\n\t"
      "ds_read_b64_tr_b16 v[138:139], v164 offset:37376\n\t"
      "v_mfma_f32_16x16x32_bf16 v[48:51], v[104:107], v[100:103], v[48:51]\n\t"
      "ds_read_b128 v[124:127], v153 offset:34816\n\t"
      "s_waitcnt lgkmcnt(8)\n\t"
      "v_mfma_f32_16x16x32_bf16 v[28:31], v[108:111], v[88:91], v[28:31]\n\t"
      "ds_read_b128 v[128:131], v153 offset:36864\n\t"
      "v_mfma_f32_16x16x32_bf16 v[36:39], v[108:111], v[92:95], v[36:39]\n\t"
      "ds_read_b128 v[132:135], v153 offset:38912\n\t"
      "v_mfma_f32_16x16x32_bf16 v[44:47], v[108:111], v[96:99], v[44:47]\n\t"
      "ds_read_b64_tr_b16 v[140:141], v165 offset:36864\n\t"
      "v_mfma_f32_16x16x32_bf16 v[52:55], v[108:111], v[100:103], v[52:55]\n\t"
      "ds_read_b64_tr_b16 v[142:143], v165 offset:37376\n\t"
      "s_waitcnt lgkmcnt(10)\n\t"
      "v_mfma_f32_16x16x32_bf16 v[56:59], v[112:115], v[88:91], v[56:59]\n\t"
      "ds_read_b64_tr_b16 v[144:145], v164 offset:45056\n\t"
      "v_mfma_f32_16x16x32_bf16 v[64:67], v[112:115], v[92:95], v[64:67]\n\t"
      "ds_read_b64_tr_b16 v[146:147], v164 offset:45568\n\t"
      "v_mfma_f32_16x16x32_bf16 v[72:75], v[112:115], v[96:99], v[72:75]\n\t"
      "ds_read_b64_tr_b16 v[148:149], v165 offset:45056\n\t"
      "v_mfma_f32_16x16x32_bf16 v[80:83], v[112:115], v[100:103], v[80:83]\n\t"
      "ds_read_b64_tr_b16 v[150:151], v165 offset:45568\n\t"
      "s_waitcnt lgkmcnt(12)\n\t"
      "v_mfma_f32_16x16x32_bf16 v[60:63], v[116:119], v[88:91], v[60:63]\n\t"
      "v_mfma_f32_16x16x32_bf16 v[68:71], v[116:119], v[92:95], v[68:71]\n\t"
      "v_mfma_f32_16x16x32_bf16 v[76:79], v[116:119], v[96:99], v[76:79]\n\t"
      "v_mfma_f32_16x16x32_bf16 v[84:87], v[116:119], v[100:103], v[84:87]\n\t"
      "s_waitcnt lgkmcnt(6)\n\t"
      "v_mfma_f32_16x16x32_bf16 v[24:27], v[136:139], v[120:123], v[24:27]\n\t"
      "s_barrier\n\t"
      "ds_read_b128 v[88:91], v156 offset:0\n\t"
      "v_mfma_f32_16x16x32_bf16 v[32:35], v[136:139], v[124:127], v[32:35]\n\t"
      "ds_read_b64_tr_b16 v[104:105], v166 offset:0\n\t"
      "v_mfma_f32_16x16x32_bf16 v[40:43], v[136:139], v[128:131], v[40:43]\n\t"
      "ds_read_b64_tr_b16 v[106:107], v166 offset:512\n\t"
      "v_mfma_f32_16x16x32_bf16 v[48:51], v[136:139], v[132:135], v[48:51]\n\t"
      "ds_read_b128 v[92:95], v156 offset:2048\n\t"
      "s_waitcnt lgkmcnt(8)\n\t"
      "v_mfma_f32_16x16x32_bf16 v[28:31], v[140:143], v[120:123], v[28:31]\n\t"
      "ds_read_b128 v[96:99], v156 offset:4096\n\t"
      "v_mfma_f32_16x16x32_bf16 v[36:39], v[140:143], v[124:127], v[36:39]\n\t"
      "ds_read_b128 v[100:103], v156 offset:6144\n\t"
      "v_mfma_f32_16x16x32_bf16 v[44:47], v[140:143], v[128:131], v[44:47]\n\t"
      "ds_read_b64_tr_b16 v[108:109], v167 offset:0\n\t"
      "v_mfma_f32_16x16x32_bf16 v[52:55], v[140:143], v[132:135], v[52:55]\n\t"
      "ds_read_b64_tr_b16 v[110:111], v167 offset:512\n\t"
      "s_waitcnt lgkmcnt(10)\n\t"
      "v_mfma_f32_16x16x32_bf16 v[56:59], v[144:147], v[120:123], v[56:59]\n\t"
      "ds_read_b64_tr_b16 v[112:113], v166 offset:8192\n\t"
      "v_mfma_f32_16x16x32_bf16 v[64:67], v[144:147], v[124:127], v[64:67]\n\t"
      "ds_read_b64_tr_b16 v[114:115], v166 offset:8704\n\t"
      "v_mfma_f32_16x16x32_bf16 v[72:75], v[144:147], v[128:131], v[72:75]\n\t"
      "ds_read_b64_tr_b16 v[116:117], v167 offset:8192\n\t"
      "v_mfma_f32_16x16x32_bf16 v[80:83], v[144:147], v[132:135], v[80:83]\n\t"
      "ds_read_b64_tr_b16 v[118:119], v167 offset:8704\n\t"
      "s_waitcnt lgkmcnt(12)\n\t"
      "v_mfma_f32_16x16x32_bf16 v[60:63], v[148:151], v[120:123], v[60:63]\n\t"
      "s_cmp_lg_u32 s52, 0\n\t"
      "s_cbranch_scc1 .Lack1086_%=\n\t"
      "s_lshl_b32 s61, %1, 7\n\t"
      "v_mov_b32 v7, 4259840\n\t"
      "s_mov_b64 exec, 1\n\t"
      "buffer_store_dwordx2 v[18:19], v7, s[48:51], s61 offen sc1\n\t"
      "s_mov_b64 exec, -1\n\t"
      ".Lack1086_%=:\n\t"
      "v_mfma_f32_16x16x32_bf16 v[68:71], v[148:151], v[124:127], v[68:71]\n\t"
      "v_mfma_f32_16x16x32_bf16 v[76:79], v[148:151], v[128:131], v[76:79]\n\t"
      "v_mfma_f32_16x16x32_bf16 v[84:87], v[148:151], v[132:135], v[84:87]\n\t"
      "s_waitcnt lgkmcnt(6)\n\t"
      "v_mfma_f32_16x16x32_bf16 v[24:27], v[104:107], v[88:91], v[24:27]\n\t"
      "ds_read_b128 v[120:123], v157 offset:0\n\t"
      "v_mfma_f32_16x16x32_bf16 v[32:35], v[104:107], v[92:95], v[32:35]\n\t"
      "ds_read_b64_tr_b16 v[136:137], v166 offset:4096\n\t"
      "v_mfma_f32_16x16x32_bf16 v[40:43], v[104:107], v[96:99], v[40:43]\n\t"
      "ds_read_b64_tr_b16 v[138:139], v166 offset:4608\n\t"
      "v_mfma_f32_16x16x32_bf16 v[48:51], v[104:107], v[100:103], v[48:51]\n\t"
      "ds_read_b128 v[124:127], v157 offset:2048\n\t"
      "s_waitcnt lgkmcnt(8)\n\t"
      "v_mfma_f32_16x16x32_bf16 v[28:31], v[108:111], v[88:91], v[28:31]\n\t"
      "ds_read_b128 v[128:131], v157 offset:4096\n\t"
      "v_mfma_f32_16x16x32_bf16 v[36:39], v[108:111], v[92:95], v[36:39]\n\t"
      "ds_read_b128 v[132:135], v157 offset:6144\n\t"
      "v_mfma_f32_16x16x32_bf16 v[44:47], v[108:111], v[96:99], v[44:47]\n\t"
      "ds_read_b64_tr_b16 v[140:141], v167 offset:4096\n\t"
      "v_mfma_f32_16x16x32_bf16 v[52:55], v[108:111], v[100:103], v[52:55]\n\t"
      "ds_read_b64_tr_b16 v[142:143], v167 offset:4608\n\t"
      "s_waitcnt lgkmcnt(10)\n\t"
      "v_mfma_f32_16x16x32_bf16 v[56:59], v[112:115], v[88:91], v[56:59]\n\t"
      "ds_read_b64_tr_b16 v[144:145], v166 offset:12288\n\t"
      "v_mfma_f32_16x16x32_bf16 v[64:67], v[112:115], v[92:95], v[64:67]\n\t"
      "ds_read_b64_tr_b16 v[146:147], v166 offset:12800\n\t"
      "v_mfma_f32_16x16x32_bf16 v[72:75], v[112:115], v[96:99], v[72:75]\n\t"
      "ds_read_b64_tr_b16 v[148:149], v167 offset:12288\n\t"
      "v_mfma_f32_16x16x32_bf16 v[80:83], v[112:115], v[100:103], v[80:83]\n\t"
      "ds_read_b64_tr_b16 v[150:151], v167 offset:12800\n\t"
      "s_waitcnt lgkmcnt(12)\n\t"
      "v_mfma_f32_16x16x32_bf16 v[60:63], v[116:119], v[88:91], v[60:63]\n\t"
      "v_mfma_f32_16x16x32_bf16 v[68:71], v[116:119], v[92:95], v[68:71]\n\t"
      "v_mfma_f32_16x16x32_bf16 v[76:79], v[116:119], v[96:99], v[76:79]\n\t"
      "v_mfma_f32_16x16x32_bf16 v[84:87], v[116:119], v[100:103], v[84:87]\n\t"
      "s_waitcnt lgkmcnt(6)\n\t"
      "v_mfma_f32_16x16x32_bf16 v[24:27], v[136:139], v[120:123], v[24:27]\n\t"
      "s_barrier\n\t"
      "ds_read_b128 v[88:91], v156 offset:32768\n\t"
      "v_mfma_f32_16x16x32_bf16 v[32:35], v[136:139], v[124:127], v[32:35]\n\t"
      "ds_read_b64_tr_b16 v[104:105], v166 offset:32768\n\t"
      "v_mfma_f32_16x16x32_bf16 v[40:43], v[136:139], v[128:131], v[40:43]\n\t"
      "ds_read_b64_tr_b16 v[106:107], v166 offset:33280\n\t"
      "v_mfma_f32_16x16x32_bf16 v[48:51], v[136:139], v[132:135], v[48:51]\n\t"
      "ds_read_b128 v[92:95], v156 offset:34816\n\t"
      "s_waitcnt lgkmcnt(8)\n\t"
      "v_mfma_f32_16x16x32_bf16 v[28:31], v[140:143], v[120:123], v[28:31]\n\t"
      "ds_read_b128 v[96:99], v156 offset:36864\n\t"
      "v_mfma_f32_16x16x32_bf16 v[36:39], v[140:143], v[124:127], v[36:39]\n\t"
      "ds_read_b128 v[100:103], v156 offset:38912\n\t"
      "v_mfma_f32_16x16x32_bf16 v[44:47], v[140:143], v[128:131], v[44:47]\n\t"
      "ds_read_b64_tr_b16 v[108:109], v167 offset:32768\n\t"
      "v_mfma_f32_16x16x32_bf16 v[52:55], v[140:143], v[132:135], v[52:55]\n\t"
      "ds_read_b64_tr_b16 v[110:111], v167 offset:33280\n\t"
      "s_waitcnt lgkmcnt(10)\n\t"
      "v_mfma_f32_16x16x32_bf16 v[56:59], v[144:147], v[120:123], v[56:59]\n\t"
      "ds_read_b64_tr_b16 v[112:113], v166 offset:40960\n\t"
      "v_mfma_f32_16x16x32_bf16 v[64:67], v[144:147], v[124:127], v[64:67]\n\t"
      "ds_read_b64_tr_b16 v[114:115], v166 offset:41472\n\t"
      "v_mfma_f32_16x16x32_bf16 v[72:75], v[144:147], v[128:131], v[72:75]\n\t"
      "ds_read_b64_tr_b16 v[116:117], v167 offset:40960\n\t"
      "v_mfma_f32_16x16x32_bf16 v[80:83], v[144:147], v[132:135], v[80:83]\n\t"
      "ds_read_b64_tr_b16 v[118:119], v167 offset:41472\n\t"
      "s_waitcnt lgkmcnt(12)\n\t"
      "v_mfma_f32_16x16x32_bf16 v[60:63], v[148:151], v[120:123], v[60:63]\n\t"
      "v_mfma_f32_16x16x32_bf16 v[68:71], v[148:151], v[124:127], v[68:71]\n\t"
      "v_mfma_f32_16x16x32_bf16 v[76:79], v[148:151], v[128:131], v[76:79]\n\t"
      "v_mfma_f32_16x16x32_bf16 v[84:87], v[148:151], v[132:135], v[84:87]\n\t"
      "s_waitcnt lgkmcnt(6)\n\t"
      "v_mfma_f32_16x16x32_bf16 v[24:27], v[104:107], v[88:91], v[24:27]\n\t"
      "ds_read_b128 v[120:123], v157 offset:32768\n\t"
      "v_mfma_f32_16x16x32_bf16 v[32:35], v[104:107], v[92:95], v[32:35]\n\t"
      "ds_read_b64_tr_b16 v[136:137], v166 offset:36864\n\t"
      "v_mfma_f32_16x16x32_bf16 v[40:43], v[104:107], v[96:99], v[40:43]\n\t"
      "ds_read_b64_tr_b16 v[138:139], v166 offset:37376\n\t"
      "v_mfma_f32_16x16x32_bf16 v[48:51], v[104:107], v[100:103], v[48:51]\n\t"
      "ds_read_b128 v[124:127], v157 offset:34816\n\t"
      "s_waitcnt lgkmcnt(8)\n\t"
      "v_mfma_f32_16x16x32_bf16 v[28:31], v[108:111], v[88:91], v[28:31]\n\t"
      "ds_read_b128 v[128:131], v157 offset:36864\n\t"
      "v_mfma_f32_16x16x32_bf16 v[36:39], v[108:111], v[92:95], v[36:39]\n\t"
      "ds_read_b128 v[132:135], v157 offset:38912\n\t"
      "v_mfma_f32_16x16x32_bf16 v[44:47], v[108:111], v[96:99], v[44:47]\n\t"
      "ds_read_b64_tr_b16 v[140:141], v167 offset:36864\n\t"
      "v_mfma_f32_16x16x32_bf16 v[52:55], v[108:111], v[100:103], v[52:55]\n\t"
      "ds_read_b64_tr_b16 v[142:143], v167 offset:37376\n\t"
      "s_waitcnt lgkmcnt(10)\n\t"
      "v_mfma_f32_16x16x32_bf16 v[56:59], v[112:115], v[88:91], v[56:59]\n\t"
      "ds_read_b64_tr_b16 v[144:145], v166 offset:45056\n\t"
      "v_mfma_f32_16x16x32_bf16 v[64:67], v[112:115], v[92:95], v[64:67]\n\t"
      "ds_read_b64_tr_b16 v[146:147], v166 offset:45568\n\t"
      "v_mfma_f32_16x16x32_bf16 v[72:75], v[112:115], v[96:99], v[72:75]\n\t"
      "ds_read_b64_tr_b16 v[148:149], v167 offset:45056\n\t"
      "v_mfma_f32_16x16x32_bf16 v[80:83], v[112:115], v[100:103], v[80:83]\n\t"
      "ds_read_b64_tr_b16 v[150:151], v167 offset:45568\n\t"
      "s_waitcnt lgkmcnt(12)\n\t"
      "v_mfma_f32_16x16x32_bf16 v[60:63], v[116:119], v[88:91], v[60:63]\n\t"
      "v_mfma_f32_16x16x32_bf16 v[68:71], v[116:119], v[92:95], v[68:71]\n\t"
      "v_mfma_f32_16x16x32_bf16 v[76:79], v[116:119], v[96:99], v[76:79]\n\t"
      "v_mfma_f32_16x16x32_bf16 v[84:87], v[116:119], v[100:103], v[84:87]\n\t"
      "s_waitcnt lgkmcnt(6)\n\t"
      "v_mfma_f32_16x16x32_bf16 v[24:27], v[136:139], v[120:123], v[24:27]\n\t"
      "s_barrier\n\t"
      "ds_read_b128 v[88:91], v160 offset:0\n\t"
      "v_mfma_f32_16x16x32_bf16 v[32:35], v[136:139], v[124:127], v[32:35]\n\t"
      "ds_read_b64_tr_b16 v[104:105], v168 offset:0\n\t"
      "v_mfma_f32_16x16x32_bf16 v[40:43], v[136:139], v[128:131], v[40:43]\n\t"
      "ds_read_b64_tr_b16 v[106:107], v168 offset:512\n\t"
      "v_mfma_f32_16x16x32_bf16 v[48:51], v[136:139], v[132:135], v[48:51]\n\t"
      "ds_read_b128 v[92:95], v160 offset:2048\n\t"
      "s_waitcnt lgkmcnt(8)\n\t"
      "v_mfma_f32_16x16x32_bf16 v[28:31], v[140:143], v[120:123], v[28:31]\n\t"
      "ds_read_b128 v[96:99], v160 offset:4096\n\t"
      "v_mfma_f32_16x16x32_bf16 v[36:39], v[140:143], v[124:127], v[36:39]\n\t"
      "ds_read_b128 v[100:103], v160 offset:6144\n\t"
      "v_mfma_f32_16x16x32_bf16 v[44:47], v[140:143], v[128:131], v[44:47]\n\t"
      "ds_read_b64_tr_b16 v[108:109], v169 offset:0\n\t"
      "v_mfma_f32_16x16x32_bf16 v[52:55], v[140:143], v[132:135], v[52:55]\n\t"
      "ds_read_b64_tr_b16 v[110:111], v169 offset:512\n\t"
      "s_waitcnt lgkmcnt(10)\n\t"
      "v_mfma_f32_16x16x32_bf16 v[56:59], v[144:147], v[120:123], v[56:59]\n\t"
      "ds_read_b64_tr_b16 v[112:113], v168 offset:8192\n\t"
      "v_mfma_f32_16x16x32_bf16 v[64:67], v[144:147], v[124:127], v[64:67]\n\t"
      "ds_read_b64_tr_b16 v[114:115], v168 offset:8704\n\t"
      "v_mfma_f32_16x16x32_bf16 v[72:75], v[144:147], v[128:131], v[72:75]\n\t"
      "ds_read_b64_tr_b16 v[116:117], v169 offset:8192\n\t"
      "v_mfma_f32_16x16x32_bf16 v[80:83], v[144:147], v[132:135], v[80:83]\n\t"
      "ds_read_b64_tr_b16 v[118:119], v169 offset:8704\n\t"
      "s_waitcnt lgkmcnt(12)\n\t"
      "v_mfma_f32_16x16x32_bf16 v[60:63], v[148:151], v[120:123], v[60:63]\n\t"
      "v_mfma_f32_16x16x32_bf16 v[68:71], v[148:151], v[124:127], v[68:71]\n\t"
      "v_mfma_f32_16x16x32_bf16 v[76:79], v[148:151], v[128:131], v[76:79]\n\t"
      "v_mfma_f32_16x16x32_bf16 v[84:87], v[148:151], v[132:135], v[84:87]\n\t"
      "s_waitcnt lgkmcnt(6)\n\t"
      "v_mfma_f32_16x16x32_bf16 v[24:27], v[104:107], v[88:91], v[24:27]\n\t"
      "ds_read_b128 v[120:123], v161 offset:0\n\t"
      "v_mfma_f32_16x16x32_bf16 v[32:35], v[104:107], v[92:95], v[32:35]\n\t"
      "ds_read_b64_tr_b16 v[136:137], v168 offset:4096\n\t"
      "v_mfma_f32_16x16x32_bf16 v[40:43], v[104:107], v[96:99], v[40:43]\n\t"
      "ds_read_b64_tr_b16 v[138:139], v168 offset:4608\n\t"
      "v_mfma_f32_16x16x32_bf16 v[48:51], v[104:107], v[100:103], v[48:51]\n\t"
      "ds_read_b128 v[124:127], v161 offset:2048\n\t"
      "s_waitcnt lgkmcnt(8)\n\t"
      "v_mfma_f32_16x16x32_bf16 v[28:31], v[108:111], v[88:91], v[28:31]\n\t"
      "ds_read_b128 v[128:131], v161 offset:4096\n\t"
      "v_mfma_f32_16x16x32_bf16 v[36:39], v[108:111], v[92:95], v[36:39]\n\t"
      "ds_read_b128 v[132:135], v161 offset:6144\n\t"
      "v_mfma_f32_16x16x32_bf16 v[44:47], v[108:111], v[96:99], v[44:47]\n\t"
      "ds_read_b64_tr_b16 v[140:141], v169 offset:4096\n\t"
      "v_mfma_f32_16x16x32_bf16 v[52:55], v[108:111], v[100:103], v[52:55]\n\t"
      "ds_read_b64_tr_b16 v[142:143], v169 offset:4608\n\t"
      "s_waitcnt lgkmcnt(10)\n\t"
      "v_mfma_f32_16x16x32_bf16 v[56:59], v[112:115], v[88:91], v[56:59]\n\t"
      "ds_read_b64_tr_b16 v[144:145], v168 offset:12288\n\t"
      "v_mfma_f32_16x16x32_bf16 v[64:67], v[112:115], v[92:95], v[64:67]\n\t"
      "ds_read_b64_tr_b16 v[146:147], v168 offset:12800\n\t"
      "v_mfma_f32_16x16x32_bf16 v[72:75], v[112:115], v[96:99], v[72:75]\n\t"
      "ds_read_b64_tr_b16 v[148:149], v169 offset:12288\n\t"
      "v_mfma_f32_16x16x32_bf16 v[80:83], v[112:115], v[100:103], v[80:83]\n\t"
      "ds_read_b64_tr_b16 v[150:151], v169 offset:12800\n\t"
      "s_waitcnt lgkmcnt(12)\n\t"
      "v_mfma_f32_16x16x32_bf16 v[60:63], v[116:119], v[88:91], v[60:63]\n\t"
      "v_mfma_f32_16x16x32_bf16 v[68:71], v[116:119], v[92:95], v[68:71]\n\t"
      "v_mfma_f32_16x16x32_bf16 v[76:79], v[116:119], v[96:99], v[76:79]\n\t"
      "v_mfma_f32_16x16x32_bf16 v[84:87], v[116:119], v[100:103], v[84:87]\n\t"
      "s_waitcnt lgkmcnt(6)\n\t"
      "v_mfma_f32_16x16x32_bf16 v[24:27], v[136:139], v[120:123], v[24:27]\n\t"
      "s_barrier\n\t"
      "ds_read_b128 v[88:91], v152 offset:0\n\t"
      "v_mfma_f32_16x16x32_bf16 v[32:35], v[136:139], v[124:127], v[32:35]\n\t"
      "ds_read_b64_tr_b16 v[104:105], v164 offset:0\n\t"
      "v_mfma_f32_16x16x32_bf16 v[40:43], v[136:139], v[128:131], v[40:43]\n\t"
      "ds_read_b64_tr_b16 v[106:107], v164 offset:512\n\t"
      "v_mfma_f32_16x16x32_bf16 v[48:51], v[136:139], v[132:135], v[48:51]\n\t"
      "ds_read_b128 v[92:95], v152 offset:2048\n\t"
      "s_waitcnt lgkmcnt(8)\n\t"
      "v_mfma_f32_16x16x32_bf16 v[28:31], v[140:143], v[120:123], v[28:31]\n\t"
      "ds_read_b128 v[96:99], v152 offset:4096\n\t"
      "v_mfma_f32_16x16x32_bf16 v[36:39], v[140:143], v[124:127], v[36:39]\n\t"
      "ds_read_b128 v[100:103], v152 offset:6144\n\t"
      "v_mfma_f32_16x16x32_bf16 v[44:47], v[140:143], v[128:131], v[44:47]\n\t"
      "ds_read_b64_tr_b16 v[108:109], v165 offset:0\n\t"
      "v_mfma_f32_16x16x32_bf16 v[52:55], v[140:143], v[132:135], v[52:55]\n\t"
      "ds_read_b64_tr_b16 v[110:111], v165 offset:512\n\t"
      "s_waitcnt lgkmcnt(10)\n\t"
      "v_mfma_f32_16x16x32_bf16 v[56:59], v[144:147], v[120:123], v[56:59]\n\t"
      "ds_read_b64_tr_b16 v[112:113], v164 offset:8192\n\t"
      "v_mfma_f32_16x16x32_bf16 v[64:67], v[144:147], v[124:127], v[64:67]\n\t"
      "ds_read_b64_tr_b16 v[114:115], v164 offset:8704\n\t"
      "v_mfma_f32_16x16x32_bf16 v[72:75], v[144:147], v[128:131], v[72:75]\n\t"
      "ds_read_b64_tr_b16 v[116:117], v165 offset:8192\n\t"
      "v_mfma_f32_16x16x32_bf16 v[80:83], v[144:147], v[132:135], v[80:83]\n\t"
      "ds_read_b64_tr_b16 v[118:119], v165 offset:8704\n\t"
      "s_waitcnt lgkmcnt(12)\n\t"
      "v_mfma_f32_16x16x32_bf16 v[60:63], v[148:151], v[120:123], v[60:63]\n\t"
      "v_mfma_f32_16x16x32_bf16 v[68:71], v[148:151], v[124:127], v[68:71]\n\t"
      "v_mfma_f32_16x16x32_bf16 v[76:79], v[148:151], v[128:131], v[76:79]\n\t"
      "v_mfma_f32_16x16x32_bf16 v[84:87], v[148:151], v[132:135], v[84:87]\n\t"
      "s_waitcnt lgkmcnt(6)\n\t"
      "v_mfma_f32_16x16x32_bf16 v[24:27], v[104:107], v[88:91], v[24:27]\n\t"
      "ds_read_b128 v[120:123], v153 offset:0\n\t"
      "v_mfma_f32_16x16x32_bf16 v[32:35], v[104:107], v[92:95], v[32:35]\n\t"
      "ds_read_b64_tr_b16 v[136:137], v164 offset:4096\n\t"
      "v_mfma_f32_16x16x32_bf16 v[40:43], v[104:107], v[96:99], v[40:43]\n\t"
      "ds_read_b64_tr_b16 v[138:139], v164 offset:4608\n\t"
      "v_mfma_f32_16x16x32_bf16 v[48:51], v[104:107], v[100:103], v[48:51]\n\t"
      "ds_read_b128 v[124:127], v153 offset:2048\n\t"
      "s_waitcnt lgkmcnt(8)\n\t"
      "v_mfma_f32_16x16x32_bf16 v[28:31], v[108:111], v[88:91], v[28:31]\n\t"
      "ds_read_b128 v[128:131], v153 offset:4096\n\t"
      "v_mfma_f32_16x16x32_bf16 v[36:39], v[108:111], v[92:95], v[36:39]\n\t"
      "ds_read_b128 v[132:135], v153 offset:6144\n\t"
      "v_mfma_f32_16x16x32_bf16 v[44:47], v[108:111], v[96:99], v[44:47]\n\t"
      "ds_read_b64_tr_b16 v[140:141], v165 offset:4096\n\t"
      "v_mfma_f32_16x16x32_bf16 v[52:55], v[108:111], v[100:103], v[52:55]\n\t"
      "ds_read_b64_tr_b16 v[142:143], v165 offset:4608\n\t"
      "s_waitcnt lgkmcnt(10)\n\t"
      "v_mfma_f32_16x16x32_bf16 v[56:59], v[112:115], v[88:91], v[56:59]\n\t"
      "ds_read_b64_tr_b16 v[144:145], v164 offset:12288\n\t"
      "v_mfma_f32_16x16x32_bf16 v[64:67], v[112:115], v[92:95], v[64:67]\n\t"
      "ds_read_b64_tr_b16 v[146:147], v164 offset:12800\n\t"
      "v_mfma_f32_16x16x32_bf16 v[72:75], v[112:115], v[96:99], v[72:75]\n\t"
      "ds_read_b64_tr_b16 v[148:149], v165 offset:12288\n\t"
      "v_mfma_f32_16x16x32_bf16 v[80:83], v[112:115], v[100:103], v[80:83]\n\t"
      "ds_read_b64_tr_b16 v[150:151], v165 offset:12800\n\t"
      "s_waitcnt lgkmcnt(12)\n\t"
      "v_mfma_f32_16x16x32_bf16 v[60:63], v[116:119], v[88:91], v[60:63]\n\t"
      "v_mfma_f32_16x16x32_bf16 v[68:71], v[116:119], v[92:95], v[68:71]\n\t"
      "v_mfma_f32_16x16x32_bf16 v[76:79], v[116:119], v[96:99], v[76:79]\n\t"
      "v_mfma_f32_16x16x32_bf16 v[84:87], v[116:119], v[100:103], v[84:87]\n\t"
      "s_waitcnt lgkmcnt(6)\n\t"
      "v_mfma_f32_16x16x32_bf16 v[24:27], v[136:139], v[120:123], v[24:27]\n\t"
      "s_barrier\n\t"
      "ds_read_b128 v[88:91], v152 offset:32768\n\t"
      "v_mfma_f32_16x16x32_bf16 v[32:35], v[136:139], v[124:127], v[32:35]\n\t"
      "ds_read_b64_tr_b16 v[104:105], v164 offset:32768\n\t"
      "v_mfma_f32_16x16x32_bf16 v[40:43], v[136:139], v[128:131], v[40:43]\n\t"
      "ds_read_b64_tr_b16 v[106:107], v164 offset:33280\n\t"
      "v_mfma_f32_16x16x32_bf16 v[48:51], v[136:139], v[132:135], v[48:51]\n\t"
      "ds_read_b128 v[92:95], v152 offset:34816\n\t"
      "s_waitcnt lgkmcnt(8)\n\t"
      "v_mfma_f32_16x16x32_bf16 v[28:31], v[140:143], v[120:123], v[28:31]\n\t"
      "ds_read_b128 v[96:99], v152 offset:36864\n\t"
      "v_mfma_f32_16x16x32_bf16 v[36:39], v[140:143], v[124:127], v[36:39]\n\t"
      "ds_read_b128 v[100:103], v152 offset:38912\n\t"
      "v_mfma_f32_16x16x32_bf16 v[44:47], v[140:143], v[128:131], v[44:47]\n\t"
      "ds_read_b64_tr_b16 v[108:109], v165 offset:32768\n\t"
      "v_mfma_f32_16x16x32_bf16 v[52:55], v[140:143], v[132:135], v[52:55]\n\t"
      "ds_read_b64_tr_b16 v[110:111], v165 offset:33280\n\t"
      "s_waitcnt lgkmcnt(10)\n\t"
      "v_mfma_f32_16x16x32_bf16 v[56:59], v[144:147], v[120:123], v[56:59]\n\t"
      "ds_read_b64_tr_b16 v[112:113], v164 offset:40960\n\t"
      "v_mfma_f32_16x16x32_bf16 v[64:67], v[144:147], v[124:127], v[64:67]\n\t"
      "ds_read_b64_tr_b16 v[114:115], v164 offset:41472\n\t"
      "v_mfma_f32_16x16x32_bf16 v[72:75], v[144:147], v[128:131], v[72:75]\n\t"
      "ds_read_b64_tr_b16 v[116:117], v165 offset:40960\n\t"
      "v_mfma_f32_16x16x32_bf16 v[80:83], v[144:147], v[132:135], v[80:83]\n\t"
      "ds_read_b64_tr_b16 v[118:119], v165 offset:41472\n\t"
      "s_waitcnt lgkmcnt(12)\n\t"
      "v_mfma_f32_16x16x32_bf16 v[60:63], v[148:151], v[120:123], v[60:63]\n\t"
      "v_mfma_f32_16x16x32_bf16 v[68:71], v[148:151], v[124:127], v[68:71]\n\t"
      "v_mfma_f32_16x16x32_bf16 v[76:79], v[148:151], v[128:131], v[76:79]\n\t"
      "v_mfma_f32_16x16x32_bf16 v[84:87], v[148:151], v[132:135], v[84:87]\n\t"
      "s_waitcnt lgkmcnt(6)\n\t"
      "v_mfma_f32_16x16x32_bf16 v[24:27], v[104:107], v[88:91], v[24:27]\n\t"
      "ds_read_b128 v[120:123], v153 offset:32768\n\t"
      "v_mfma_f32_16x16x32_bf16 v[32:35], v[104:107], v[92:95], v[32:35]\n\t"
      "ds_read_b64_tr_b16 v[136:137], v164 offset:36864\n\t"
      "v_mfma_f32_16x16x32_bf16 v[40:43], v[104:107], v[96:99], v[40:43]\n\t"
      "ds_read_b64_tr_b16 v[138:139], v164 offset:37376\n\t"
      "v_mfma_f32_16x16x32_bf16 v[48:51], v[104:107], v[100:103], v[48:51]\n\t"
      "ds_read_b128 v[124:127], v153 offset:34816\n\t"
      "s_waitcnt lgkmcnt(8)\n\t"
      "v_mfma_f32_16x16x32_bf16 v[28:31], v[108:111], v[88:91], v[28:31]\n\t"
      "ds_read_b128 v[128:131], v153 offset:36864\n\t"
      "v_mfma_f32_16x16x32_bf16 v[36:39], v[108:111], v[92:95], v[36:39]\n\t"
      "ds_read_b128 v[132:135], v153 offset:38912\n\t"
      "v_mfma_f32_16x16x32_bf16 v[44:47], v[108:111], v[96:99], v[44:47]\n\t"
      "ds_read_b64_tr_b16 v[140:141], v165 offset:36864\n\t"
      "v_mfma_f32_16x16x32_bf16 v[52:55], v[108:111], v[100:103], v[52:55]\n\t"
      "ds_read_b64_tr_b16 v[142:143], v165 offset:37376\n\t"
      "s_waitcnt lgkmcnt(10)\n\t"
      "v_mfma_f32_16x16x32_bf16 v[56:59], v[112:115], v[88:91], v[56:59]\n\t"
      "ds_read_b64_tr_b16 v[144:145], v164 offset:45056\n\t"
      "v_mfma_f32_16x16x32_bf16 v[64:67], v[112:115], v[92:95], v[64:67]\n\t"
      "ds_read_b64_tr_b16 v[146:147], v164 offset:45568\n\t"
      "v_mfma_f32_16x16x32_bf16 v[72:75], v[112:115], v[96:99], v[72:75]\n\t"
      "ds_read_b64_tr_b16 v[148:149], v165 offset:45056\n\t"
      "v_mfma_f32_16x16x32_bf16 v[80:83], v[112:115], v[100:103], v[80:83]\n\t"
      "ds_read_b64_tr_b16 v[150:151], v165 offset:45568\n\t"
      "s_waitcnt lgkmcnt(12)\n\t"
      "v_mfma_f32_16x16x32_bf16 v[60:63], v[116:119], v[88:91], v[60:63]\n\t"
      "v_mfma_f32_16x16x32_bf16 v[68:71], v[116:119], v[92:95], v[68:71]\n\t"
      "v_mfma_f32_16x16x32_bf16 v[76:79], v[116:119], v[96:99], v[76:79]\n\t"
      "v_mfma_f32_16x16x32_bf16 v[84:87], v[116:119], v[100:103], v[84:87]\n\t"
      "s_waitcnt lgkmcnt(6)\n\t"
      "v_mfma_f32_16x16x32_bf16 v[24:27], v[136:139], v[120:123], v[24:27]\n\t"
      "s_barrier\n\t"
      "ds_read_b128 v[88:91], v156 offset:0\n\t"
      "v_mfma_f32_16x16x32_bf16 v[32:35], v[136:139], v[124:127], v[32:35]\n\t"
      "ds_read_b64_tr_b16 v[104:105], v166 offset:0\n\t"
      "v_mfma_f32_16x16x32_bf16 v[40:43], v[136:139], v[128:131], v[40:43]\n\t"
      "ds_read_b64_tr_b16 v[106:107], v166 offset:512\n\t"
      "v_mfma_f32_16x16x32_bf16 v[48:51], v[136:139], v[132:135], v[48:51]\n\t"
      "ds_read_b128 v[92:95], v156 offset:2048\n\t"
      "s_waitcnt lgkmcnt(8)\n\t"
      "v_mfma_f32_16x16x32_bf16 v[28:31], v[140:143], v[120:123], v[28:31]\n\t"
      "ds_read_b128 v[96:99], v156 offset:4096\n\t"
      "v_mfma_f32_16x16x32_bf16 v[36:39], v[140:143], v[124:127], v[36:39]\n\t"
      "ds_read_b128 v[100:103], v156 offset:6144\n\t"
      "v_mfma_f32_16x16x32_bf16 v[44:47], v[140:143], v[128:131], v[44:47]\n\t"
      "ds_read_b64_tr_b16 v[108:109], v167 offset:0\n\t"
      "v_mfma_f32_16x16x32_bf16 v[52:55], v[140:143], v[132:135], v[52:55]\n\t"
      "ds_read_b64_tr_b16 v[110:111], v167 offset:512\n\t"
      "s_waitcnt lgkmcnt(10)\n\t"
      "v_mfma_f32_16x16x32_bf16 v[56:59], v[144:147], v[120:123], v[56:59]\n\t"
      "ds_read_b64_tr_b16 v[112:113], v166 offset:8192\n\t"
      "v_mfma_f32_16x16x32_bf16 v[64:67], v[144:147], v[124:127], v[64:67]\n\t"
      "ds_read_b64_tr_b16 v[114:115], v166 offset:8704\n\t"
      "v_mfma_f32_16x16x32_bf16 v[72:75], v[144:147], v[128:131], v[72:75]\n\t"
      "ds_read_b64_tr_b16 v[116:117], v167 offset:8192\n\t"
      "v_mfma_f32_16x16x32_bf16 v[80:83], v[144:147], v[132:135], v[80:83]\n\t"
      "ds_read_b64_tr_b16 v[118:119], v167 offset:8704\n\t"
      "s_waitcnt lgkmcnt(12)\n\t"
      "v_mfma_f32_16x16x32_bf16 v[60:63], v[148:151], v[120:123], v[60:63]\n\t"
      "v_mfma_f32_16x16x32_bf16 v[68:71], v[148:151], v[124:127], v[68:71]\n\t"
      "v_mfma_f32_16x16x32_bf16 v[76:79], v[148:151], v[128:131], v[76:79]\n\t"
      "v_mfma_f32_16x16x32_bf16 v[84:87], v[148:151], v[132:135], v[84:87]\n\t"
      "s_waitcnt lgkmcnt(0)\n\t"
      "v_mfma_f32_16x16x32_bf16 v[24:27], v[104:107], v[88:91], v[24:27]\n\t"
      "ds_read_b64_tr_b16 v[136:137], v166 offset:4096\n\t"
      "ds_read_b64_tr_b16 v[138:139], v166 offset:4608\n\t"
      "v_mfma_f32_16x16x32_bf16 v[28:31], v[108:111], v[88:91], v[28:31]\n\t"
      "ds_read_b64_tr_b16 v[140:141], v167 offset:4096\n\t"
      "ds_read_b64_tr_b16 v[142:143], v167 offset:4608\n\t"
      "v_mfma_f32_16x16x32_bf16 v[56:59], v[112:115], v[88:91], v[56:59]\n\t"
      "ds_read_b64_tr_b16 v[144:145], v166 offset:12288\n\t"
      "ds_read_b64_tr_b16 v[146:147], v166 offset:12800\n\t"
      "v_mfma_f32_16x16x32_bf16 v[60:63], v[116:119], v[88:91], v[60:63]\n\t"
      "ds_read_b64_tr_b16 v[148:149], v167 offset:12288\n\t"
      "ds_read_b64_tr_b16 v[150:151], v167 offset:12800\n\t"
      "v_mfma_f32_16x16x32_bf16 v[32:35], v[104:107], v[92:95], v[32:35]\n\t"
      "ds_read_b128 v[120:123], v157 offset:0\n\t"
      "ds_read_b128 v[124:127], v157 offset:2048\n\t"
      "v_mfma_f32_16x16x32_bf16 v[36:39], v[108:111], v[92:95], v[36:39]\n\t"
      "ds_read_b128 v[128:131], v157 offset:4096\n\t"
      "ds_read_b128 v[132:135], v157 offset:6144\n\t"
      "v_mfma_f32_16x16x32_bf16 v[64:67], v[112:115], v[92:95], v[64:67]\n\t"
      "v_mfma_f32_16x16x32_bf16 v[68:71], v[116:119], v[92:95], v[68:71]\n\t"
      "v_mfma_f32_16x16x32_bf16 v[40:43], v[104:107], v[96:99], v[40:43]\n\t"
      "v_mfma_f32_16x16x32_bf16 v[44:47], v[108:111], v[96:99], v[44:47]\n\t"
      "v_mfma_f32_16x16x32_bf16 v[72:75], v[112:115], v[96:99], v[72:75]\n\t"
      "v_mfma_f32_16x16x32_bf16 v[76:79], v[116:119], v[96:99], v[76:79]\n\t"
      "s_waitcnt lgkmcnt(0)\n\t"
      "v_mfma_f32_16x16x32_bf16 v[24:27], v[136:139], v[120:123], v[24:27]\n\t"
      "v_mfma_f32_16x16x32_bf16 v[28:31], v[140:143], v[120:123], v[28:31]\n\t"
      "v_mfma_f32_16x16x32_bf16 v[56:59], v[144:147], v[120:123], v[56:59]\n\t"
      "v_mfma_f32_16x16x32_bf16 v[60:63], v[148:151], v[120:123], v[60:63]\n\t"
      "v_mfma_f32_16x16x32_bf16 v[32:35], v[136:139], v[124:127], v[32:35]\n\t"
      "v_mfma_f32_16x16x32_bf16 v[36:39], v[140:143], v[124:127], v[36:39]\n\t"
      "s_nop 5\n\t"
      "v_pk_mul_f32 v[176:177], v[24:25], s[76:77]\n\t"
      "v_pk_mul_f32 v[178:179], v[26:27], s[76:77]\n\t"
      "v_mfma_f32_16x16x32_bf16 v[64:67], v[144:147], v[124:127], v[64:67]\n\t"
      "v_pk_mul_f32 v[180:181], v[28:29], s[76:77]\n\t"
      "v_pk_mul_f32 v[182:183], v[30:31], s[76:77]\n\t"
      "v_mfma_f32_16x16x32_bf16 v[68:71], v[148:151], v[124:127], v[68:71]\n\t"
      "v_exp_f32 v176, v176\n\t"
      "v_mfma_f32_16x16x32_bf16 v[40:43], v[136:139], v[128:131], v[40:43]\n\t"
      "v_exp_f32 v177, v177\n\t"
      "v_mfma_f32_16x16x32_bf16 v[44:47], v[140:143], v[128:131], v[44:47]\n\t"
      "v_exp_f32 v178, v178\n\t"
      "v_mfma_f32_16x16x32_bf16 v[72:75], v[144:147], v[128:131], v[72:75]\n\t"
      "v_exp_f32 v179, v179\n\t"
      "v_mfma_f32_16x16x32_bf16 v[76:79], v[148:151], v[128:131], v[76:79]\n\t"
      "v_exp_f32 v180, v180\n\t"
      "v_mfma_f32_16x16x32_bf16 v[48:51], v[104:107], v[100:103], v[48:51]\n\t"
      "v_exp_f32 v181, v181\n\t"
      "v_mfma_f32_16x16x32_bf16 v[52:55], v[108:111], v[100:103], v[52:55]\n\t"
      "v_exp_f32 v182, v182\n\t"
      "v_mfma_f32_16x16x32_bf16 v[80:83], v[112:115], v[100:103], v[80:83]\n\t"
      "v_exp_f32 v183, v183\n\t"
      "v_mfma_f32_16x16x32_bf16 v[84:87], v[116:119], v[100:103], v[84:87]\n\t"
      "v_pk_mul_f32 v[184:185], v[24:25], v[56:57]\n\t"
      "v_pk_mul_f32 v[186:187], v[26:27], v[58:59]\n\t"
      "v_mfma_f32_16x16x32_bf16 v[48:51], v[136:139], v[132:135], v[48:51]\n\t"
      "v_pk_mul_f32 v[188:189], v[28:29], v[60:61]\n\t"
      "v_pk_mul_f32 v[190:191], v[30:31], v[62:63]\n\t"
      "v_mfma_f32_16x16x32_bf16 v[52:55], v[140:143], v[132:135], v[52:55]\n\t"
      "v_pk_add_f32 v[176:177], v[176:177], 1.0 op_sel_hi:[1,0]\n\t"
      "v_pk_add_f32 v[178:179], v[178:179], 1.0 op_sel_hi:[1,0]\n\t"
      "v_mfma_f32_16x16x32_bf16 v[80:83], v[144:147], v[132:135], v[80:83]\n\t"
      "v_pk_add_f32 v[180:181], v[180:181], 1.0 op_sel_hi:[1,0]\n\t"
      "v_pk_add_f32 v[182:183], v[182:183], 1.0 op_sel_hi:[1,0]\n\t"
      "v_mfma_f32_16x16x32_bf16 v[84:87], v[148:151], v[132:135], v[84:87]\n\t"
      "v_rcp_f32 v176, v176\n\t"
      "v_rcp_f32 v177, v177\n\t"
      "v_rcp_f32 v178, v178\n\t"
      "v_rcp_f32 v179, v179\n\t"
      "v_rcp_f32 v180, v180\n\t"
      "v_rcp_f32 v181, v181\n\t"
      "v_rcp_f32 v182, v182\n\t"
      "v_rcp_f32 v183, v183\n\t"
      "s_nop 0\n\t"
      "v_pk_mul_f32 v[184:185], v[184:185], v[176:177]\n\t"
      "v_pk_mul_f32 v[186:187], v[186:187], v[178:179]\n\t"
      "v_pk_mul_f32 v[188:189], v[188:189], v[180:181]\n\t"
      "v_pk_mul_f32 v[190:191], v[190:191], v[182:183]\n\t"
      "v_cvt_pk_bf16_f32 v192, v184, v185\n\t"
      "v_cvt_pk_bf16_f32 v193, v186, v187\n\t"
      "v_cvt_pk_bf16_f32 v194, v188, v189\n\t"
      "v_cvt_pk_bf16_f32 v195, v190, v191\n\t"
      "buffer_store_dwordx4 v[192:195], v16, s[48:51], s78 offen offset:0 sc1\n\t"
      "v_pk_mul_f32 v[196:197], v[32:33], s[76:77]\n\t"
      "v_pk_mul_f32 v[198:199], v[34:35], s[76:77]\n\t"
      "v_pk_mul_f32 v[200:201], v[36:37], s[76:77]\n\t"
      "v_pk_mul_f32 v[202:203], v[38:39], s[76:77]\n\t"
      "v_exp_f32 v196, v196\n\t"
      "v_exp_f32 v197, v197\n\t"
      "v_exp_f32 v198, v198\n\t"
      "v_exp_f32 v199, v199\n\t"
      "v_exp_f32 v200, v200\n\t"
      "v_exp_f32 v201, v201\n\t"
      "v_exp_f32 v202, v202\n\t"
      "v_exp_f32 v203, v203\n\t"
      "v_pk_mul_f32 v[204:205], v[32:33], v[64:65]\n\t"
      "v_pk_mul_f32 v[206:207], v[34:35], v[66:67]\n\t"
      "v_pk_mul_f32 v[208:209], v[36:37], v[68:69]\n\t"
      "v_pk_mul_f32 v[210:211], v[38:39], v[70:71]\n\t"
      "v_pk_add_f32 v[196:197], v[196:197], 1.0 op_sel_hi:[1,0]\n\t"
      "v_pk_add_f32 v[198:199], v[198:199], 1.0 op_sel_hi:[1,0]\n\t"
      "v_pk_add_f32 v[200:201], v[200:201], 1.0 op_sel_hi:[1,0]\n\t"
      "v_pk_add_f32 v[202:203], v[202:203], 1.0 op_sel_hi:[1,0]\n\t"
      "v_rcp_f32 v196, v196\n\t"
      "v_rcp_f32 v197, v197\n\t"
      "v_rcp_f32 v198, v198\n\t"
      "v_rcp_f32 v199, v199\n\t"
      "v_rcp_f32 v200, v200\n\t"
      "v_rcp_f32 v201, v201\n\t"
      "v_rcp_f32 v202, v202\n\t"
      "v_rcp_f32 v203, v203\n\t"
      "s_nop 0\n\t"
      "v_pk_mul_f32 v[204:205], v[204:205], v[196:197]\n\t"
      "v_pk_mul_f32 v[206:207], v[206:207], v[198:199]\n\t"
      "v_pk_mul_f32 v[208:209], v[208:209], v[200:201]\n\t"
      "v_pk_mul_f32 v[210:211], v[210:211], v[202:203]\n\t"
      "v_cvt_pk_bf16_f32 v212, v204, v205\n\t"
      "v_cvt_pk_bf16_f32 v213, v206, v207\n\t"
      "v_cvt_pk_bf16_f32 v214, v208, v209\n\t"
      "v_cvt_pk_bf16_f32 v215, v210, v211\n\t"
      "buffer_store_dwordx4 v[212:215], v16, s[48:51], s78 offen offset:1024 sc1\n\t"
      "v_pk_mul_f32 v[216:217], v[40:41], s[76:77]\n\t"
      "v_pk_mul_f32 v[218:219], v[42:43], s[76:77]\n\t"
      "v_pk_mul_f32 v[220:221], v[44:45], s[76:77]\n\t"
      "v_pk_mul_f32 v[222:223], v[46:47], s[76:77]\n\t"
      "v_exp_f32 v216, v216\n\t"
      "v_exp_f32 v217, v217\n\t"
      "v_exp_f32 v218, v218\n\t"
      "v_exp_f32 v219, v219\n\t"
      "v_exp_f32 v220, v220\n\t"
      "v_exp_f32 v221, v221\n\t"
      "v_exp_f32 v222, v222\n\t"
      "v_exp_f32 v223, v223\n\t"
      "v_pk_mul_f32 v[224:225], v[40:41], v[72:73]\n\t"
      "v_pk_mul_f32 v[226:227], v[42:43], v[74:75]\n\t"
      "v_pk_mul_f32 v[228:229], v[44:45], v[76:77]\n\t"
      "v_pk_mul_f32 v[230:231], v[46:47], v[78:79]\n\t"
      "v_pk_add_f32 v[216:217], v[216:217], 1.0 op_sel_hi:[1,0]\n\t"
      "v_pk_add_f32 v[218:219], v[218:219], 1.0 op_sel_hi:[1,0]\n\t"
      "v_pk_add_f32 v[220:221], v[220:221], 1.0 op_sel_hi:[1,0]\n\t"
      "v_pk_add_f32 v[222:223], v[222:223], 1.0 op_sel_hi:[1,0]\n\t"
      "v_rcp_f32 v216, v216\n\t"
      "v_rcp_f32 v217, v217\n\t"
      "v_rcp_f32 v218, v218\n\t"
      "v_rcp_f32 v219, v219\n\t"
      "v_rcp_f32 v220, v220\n\t"
      "v_rcp_f32 v221, v221\n\t"
      "v_rcp_f32 v222, v222\n\t"
      "v_rcp_f32 v223, v223\n\t"
      "s_nop 0\n\t"
      "v_pk_mul_f32 v[224:225], v[224:225], v[216:217]\n\t"
      "v_pk_mul_f32 v[226:227], v[226:227], v[218:219]\n\t"
      "v_pk_mul_f32 v[228:229], v[228:229], v[220:221]\n\t"
      "v_pk_mul_f32 v[230:231], v[230:231], v[222:223]\n\t"
      "v_cvt_pk_bf16_f32 v232, v224, v225\n\t"
      "v_cvt_pk_bf16_f32 v233, v226, v227\n\t"
      "v_cvt_pk_bf16_f32 v234, v228, v229\n\t"
      "v_cvt_pk_bf16_f32 v235, v230, v231\n\t"
      "buffer_store_dwordx4 v[232:235], v16, s[48:51], s66 offen offset:0 sc1\n\t"
      "v_pk_mul_f32 v[236:237], v[48:49], s[76:77]\n\t"
      "v_pk_mul_f32 v[238:239], v[50:51], s[76:77]\n\t"
      "v_pk_mul_f32 v[240:241], v[52:53], s[76:77]\n\t"
      "v_pk_mul_f32 v[242:243], v[54:55], s[76:77]\n\t"
      "v_exp_f32 v236, v236\n\t"
      "v_exp_f32 v237, v237\n\t"
      "v_exp_f32 v238, v238\n\t"
      "v_exp_f32 v239, v239\n\t"
      "v_exp_f32 v240, v240\n\t"
      "v_exp_f32 v241, v241\n\t"
      "v_exp_f32 v242, v242\n\t"
      "v_exp_f32 v243, v243\n\t"
      "v_pk_mul_f32 v[244:245], v[48:49], v[80:81]\n\t"
      "v_pk_mul_f32 v[246:247], v[50:51], v[82:83]\n\t"
      "v_pk_mul_f32 v[248:249], v[52:53], v[84:85]\n\t"
      "v_pk_mul_f32 v[250:251], v[54:55], v[86:87]\n\t"
      "v_pk_add_f32 v[236:237], v[236:237], 1.0 op_sel_hi:[1,0]\n\t"
      "v_pk_add_f32 v[238:239], v[238:239], 1.0 op_sel_hi:[1,0]\n\t"
      "v_pk_add_f32 v[240:241], v[240:241], 1.0 op_sel_hi:[1,0]\n\t"
      "v_pk_add_f32 v[242:243], v[242:243], 1.0 op_sel_hi:[1,0]\n\t"
      "v_rcp_f32 v236, v236\n\t"
      "v_rcp_f32 v237, v237\n\t"
      "v_rcp_f32 v238, v238\n\t"
      "v_rcp_f32 v239, v239\n\t"
      "v_rcp_f32 v240, v240\n\t"
      "v_rcp_f32 v241, v241\n\t"
      "v_rcp_f32 v242, v242\n\t"
      "v_rcp_f32 v243, v243\n\t"
      "s_nop 0\n\t"
      "v_pk_mul_f32 v[244:245], v[244:245], v[236:237]\n\t"
      "v_pk_mul_f32 v[246:247], v[246:247], v[238:239]\n\t"
      "v_pk_mul_f32 v[248:249], v[248:249], v[240:241]\n\t"
      "v_pk_mul_f32 v[250:251], v[250:251], v[242:243]\n\t"
      "v_cvt_pk_bf16_f32 v252, v244, v245\n\t"
      "v_cvt_pk_bf16_f32 v253, v246, v247\n\t"
      "v_cvt_pk_bf16_f32 v254, v248, v249\n\t"
      "v_cvt_pk_bf16_f32 v255, v250, v251\n\t"
      "buffer_store_dwordx4 v[252:255], v16, s[48:51], s66 offen offset:1024 sc1\n\t"
      "s_barrier\n\t"
      "s_branch .Lp2%=\n\t"
      ".Lnoprod%=:\n\t"
      "s_and_b32 s59, s53, 1\n\t"
      "s_lshl_b32 s59, s59, 2\n\t"
      "s_lshr_b32 s60, s53, 1\n\t"
      "s_add_u32 s59, s59, s60\n\t"
      "s_lshl_b32 s59, s59, 2\n\t"
      "s_add_u32 s59, s59, s54\n\t"
      "s_sub_u32 s59, s59, 28\n\t"
      "s_lshl3_add_u32 s59, s59, s52\n\t"
      "s_lshl_b32 s59, s59, 12\n\t"
      "s_add_u32 s16, s16, s59\n\t"
      "s_addc_u32 s17, s17, 0\n\t"
      "v_lshrrev_b32 v49, 4, v1\n\t"
      "v_and_b32 v48, 15, v1\n\t"
      "v_lshlrev_b32 v48, 4, v48\n\t"
      "v_lshl_add_u32 v48, v49, 10, v48\n\t"
      "s_bitcmp1_b32 s53, 0\n\t"
      "s_cbranch_scc1 .Lxp1639_%=\n\t"
      "global_load_dwordx4 v[52:55], v48, s[16:17] offset:0\n\t"
      "global_load_dwordx4 v[56:59], v48, s[16:17] offset:256\n\t"
      "global_load_dwordx4 v[60:63], v48, s[16:17] offset:512\n\t"
      "global_load_dwordx4 v[64:67], v48, s[16:17] offset:768\n\t"
      "v_add_u32 v50, 0x80000, v48\n\t"
      "global_load_dwordx4 v[52:55], v50, s[16:17] offset:0\n\t"
      "global_load_dwordx4 v[56:59], v50, s[16:17] offset:256\n\t"
      "global_load_dwordx4 v[60:63], v50, s[16:17] offset:512\n\t"
      "global_load_dwordx4 v[64:67], v50, s[16:17] offset:768\n\t"
      ".Lxp1639_%=:\n\t"
      "s_waitcnt vmcnt(0)\n\t"
      "s_waitcnt vmcnt(0)\n\t"
      "v_readfirstlane_b32 s64, v18\n\t"
      "v_readfirstlane_b32 s65, v19\n\t"
      "s_nop 3\n\t"
      "s_add_u32 s64, s64, 1\n\t"
      "s_addc_u32 s65, s65, 0\n\t"
      "v_mov_b32 v18, s64\n\t"
      "v_mov_b32 v19, s65\n\t"
      "s_barrier\n\t"
      "s_cmp_lg_u32 s52, 0\n\t"
      "s_cbranch_scc1 .Lack1661_%=\n\t"
      "s_lshl_b32 s61, %1, 7\n\t"
      "v_mov_b32 v7, 4259840\n\t"
      "s_mov_b64 exec, 1\n\t"
      "buffer_store_dwordx2 v[18:19], v7, s[48:51], s61 offen sc1\n\t"
      "s_mov_b64 exec, -1\n\t"
      ".Lack1661_%=:\n\t"
      ".Lp2%=:\n\t"
      "ds_read_b64_tr_b16 v[56:57], v10 offset:32768\n\t"
      "ds_read_b64_tr_b16 v[58:59], v10 offset:34816\n\t"
      "ds_read_b64_tr_b16 v[60:61], v11 offset:32768\n\t"
      "ds_read_b64_tr_b16 v[62:63], v11 offset:34816\n\t"
      "ds_read_b64_tr_b16 v[64:65], v12 offset:32768\n\t"
      "ds_read_b64_tr_b16 v[66:67], v12 offset:34816\n\t"
      "ds_read_b64_tr_b16 v[68:69], v13 offset:32768\n\t"
      "ds_read_b64_tr_b16 v[70:71], v13 offset:34816\n\t"
      "ds_read_b64_tr_b16 v[72:73], v10 offset:36864\n\t"
      "ds_read_b64_tr_b16 v[74:75], v10 offset:38912\n\t"
      "ds_read_b64_tr_b16 v[76:77], v11 offset:36864\n\t"
      "ds_read_b64_tr_b16 v[78:79], v11 offset:38912\n\t"
      "ds_read_b64_tr_b16 v[80:81], v12 offset:36864\n\t"
      "ds_read_b64_tr_b16 v[82:83], v12 offset:38912\n\t"
      "ds_read_b64_tr_b16 v[84:85], v13 offset:36864\n\t"
      "ds_read_b64_tr_b16 v[86:87], v13 offset:38912\n\t"
      "v_add_u32 v10, 0x10000, v10\n\t"
      "v_add_u32 v11, 0x10000, v11\n\t"
      "v_add_u32 v12, 0x10000, v12\n\t"
      "v_add_u32 v13, 0x10000, v13\n\t"
      "ds_read_b64_tr_b16 v[88:89], v10 offset:0\n\t"
      "ds_read_b64_tr_b16 v[90:91], v10 offset:2048\n\t"
      "ds_read_b64_tr_b16 v[92:93], v11 offset:0\n\t"
      "ds_read_b64_tr_b16 v[94:95], v11 offset:2048\n\t"
      "ds_read_b64_tr_b16 v[96:97], v12 offset:0\n\t"
      "ds_read_b64_tr_b16 v[98:99], v12 offset:2048\n\t"
      "ds_read_b64_tr_b16 v[100:101], v13 offset:0\n\t"
      "ds_read_b64_tr_b16 v[102:103], v13 offset:2048\n\t"
      "ds_read_b64_tr_b16 v[104:105], v10 offset:4096\n\t"
      "ds_read_b64_tr_b16 v[106:107], v10 offset:6144\n\t"
      "ds_read_b64_tr_b16 v[108:109], v11 offset:4096\n\t"
      "ds_read_b64_tr_b16 v[110:111], v11 offset:6144\n\t"
      "ds_read_b64_tr_b16 v[112:113], v12 offset:4096\n\t"
      "ds_read_b64_tr_b16 v[114:115], v12 offset:6144\n\t"
      "ds_read_b64_tr_b16 v[116:117], v13 offset:4096\n\t"
      "ds_read_b64_tr_b16 v[118:119], v13 offset:6144\n\t"
      "v_add_u32 v10, 0xfffe0000, v10\n\t"
      "v_add_u32 v11, 0xfffe0000, v11\n\t"
      "v_add_u32 v12, 0xfffe0000, v12\n\t"
      "v_add_u32 v13, 0xfffe0000, v13\n\t"
      "v_lshlrev_b32 v7, 3, v1\n\t"
      "v_add_u32 v7, 4194304, v7\n\t"
      "s_cmp_ge_u32 s54, 28\n\t"
      "s_cbranch_scc1 .Lnoflag%=\n\t"
      "s_waitcnt vmcnt(0)\n\t"
      "s_mov_b64 exec, 0xff\n\t"
      "buffer_store_dwordx2 v[18:19], v7, s[48:51], s67 offen sc1\n\t"
      "s_mov_b64 exec, -1\n\t"
      ".Lnoflag%=:\n\t"
      "s_waitcnt lgkmcnt(0)\n\t"
      "s_barrier\n\t"
      "s_mov_b32 s72, 0\n\t"
      "s_barrier\n\t"
      "s_mov_b64 exec, 0x3fff\n\t"
      ".Lpoll%=:\n\t"
      "buffer_load_dwordx2 v[2:3], v17, s[48:51], 0 offen sc1\n\t"
      "s_waitcnt vmcnt(0)\n\t"
      "v_cmp_eq_u64 vcc, s[64:65], v[2:3]\n\t"
      "s_nop 3\n\t"
      "s_cmp_eq_u64 vcc, exec\n\t"
      "s_cbranch_scc1 .Lpolldone%=\n\t"
      "s_sleep 1\n\t"
      "s_add_u32 s72, s72, 1\n\t"
      "s_cmp_lt_u32 s72, 0x8000\n\t"
      "s_cbranch_scc1 .Lpoll%=\n\t"
      ".Lpolldone%=:\n\t"
      "s_mov_b64 exec, -1\n\t"
      "s_lshl_b32 s78, s73, 12\n\t"
      "s_add_u32 s78, s78, s93\n\t"
      "s_add_u32 s73, s73, 1\n\t"
      "s_cmp_eq_u32 s73, 7\n\t"
      "s_cselect_b32 s73, 0, s73\n\t"
      "buffer_load_dwordx4 v[144:147], v16, s[48:51], s78 offen offset:0 sc1\n\t"
      "buffer_load_dwordx4 v[148:151], v16, s[48:51], s78 offen offset:1024 sc1\n\t"
      "buffer_load_dwordx4 v[152:155], v16, s[48:51], s78 offen offset:2048 sc1\n\t"
      "buffer_load_dwordx4 v[156:159], v16, s[48:51], s78 offen offset:3072 sc1\n\t"
      "s_lshl_b32 s78, s73, 12\n\t"
      "s_add_u32 s78, s78, s93\n\t"
      "s_add_u32 s73, s73, 1\n\t"
      "s_cmp_eq_u32 s73, 7\n\t"
      "s_cselect_b32 s73, 0, s73\n\t"
      "buffer_load_dwordx4 v[160:163], v16, s[48:51], s78 offen offset:0 sc1\n\t"
      "buffer_load_dwordx4 v[164:167], v16, s[48:51], s78 offen offset:1024 sc1\n\t"
      "buffer_load_dwordx4 v[168:171], v16, s[48:51], s78 offen offset:2048 sc1\n\t"
      "buffer_load_dwordx4 v[172:175], v16, s[48:51], s78 offen offset:3072 sc1\n\t"
      "s_lshl_b32 s78, s73, 12\n\t"
      "s_add_u32 s78, s78, s93\n\t"
      "s_add_u32 s73, s73, 1\n\t"
      "s_cmp_eq_u32 s73, 7\n\t"
      "s_cselect_b32 s73, 0, s73\n\t"
      "buffer_load_dwordx4 v[176:179], v16, s[48:51], s78 offen offset:0 sc1\n\t"
      "buffer_load_dwordx4 v[180:183], v16, s[48:51], s78 offen offset:1024 sc1\n\t"
      "v_lshrrev_b32 v6, 4, v1\n\t"
      "v_and_b32 v7, 15, v1\n\t"
      "v_and_b32 v8, 7, v1\n\t"
      "v_xor_b32 v6, v6, v8\n\t"
      "s_lshl_b32 s61, s52, 13\n\t"
      "v_lshlrev_b32 v7, 8, v7\n\t"
      "v_add_u32 v7, s61, v7\n\t"
      "v_xor_b32 v9, 0, v6\n\t"
      "v_lshl_add_u32 v2, v9, 4, v7\n\t"
      "v_xor_b32 v9, 4, v6\n\t"
      "v_lshl_add_u32 v3, v9, 4, v7\n\t"
      "v_lshrrev_b32 v6, 3, v1\n\t"
      "v_lshlrev_b32 v8, 1, v8\n\t"
      "v_xor_b32 v8, v8, v6\n\t"
      "s_lshl_b32 s61, s52, 11\n\t"
      "v_lshlrev_b32 v7, 8, v6\n\t"
      "v_add_u32 v7, s61, v7\n\t"
      "v_lshl_add_u32 v7, v8, 4, v7\n\t"
      "v_xor_b32 v8, 16, v7\n\t"
      "v_and_b32 v9, 7, v1\n\t"
      "v_lshlrev_b32 v9, 4, v9\n\t"
      "v_lshl_add_u32 v9, v6, 10, v9\n\t"
      "s_lshl_b32 s59, s57, 15\n\t"
      "s_lshl_b32 s60, s52, 13\n\t"
      "s_add_u32 s59, s59, s60\n\t"
      "s_lshl_b32 s60, s58, 7\n\t"
      "s_add_u32 s59, s59, s60\n\t"
      "s_waitcnt vmcnt(9)\n\t"
      "v_mfma_f32_16x16x32_bf16 v[24:27], v[56:59], v[144:147], 0\n\t"
      "v_mfma_f32_16x16x32_bf16 v[32:35], v[60:63], v[144:147], 0\n\t"
      "buffer_load_dwordx4 v[184:187], v16, s[48:51], s78 offen offset:2048 sc1\n\t"
      "v_mfma_f32_16x16x32_bf16 v[40:43], v[64:67], v[144:147], 0\n\t"
      "v_mfma_f32_16x16x32_bf16 v[48:51], v[68:71], v[144:147], 0\n\t"
      "s_waitcnt vmcnt(9)\n\t"
      "v_mfma_f32_16x16x32_bf16 v[28:31], v[56:59], v[148:151], 0\n\t"
      "v_mfma_f32_16x16x32_bf16 v[36:39], v[60:63], v[148:151], 0\n\t"
      "buffer_load_dwordx4 v[188:191], v16, s[48:51], s78 offen offset:3072 sc1\n\t"
      "v_mfma_f32_16x16x32_bf16 v[44:47], v[64:67], v[148:151], 0\n\t"
      "v_mfma_f32_16x16x32_bf16 v[52:55], v[68:71], v[148:151], 0\n\t"
      "s_waitcnt vmcnt(9)\n\t"
      "v_mfma_f32_16x16x32_bf16 v[24:27], v[72:75], v[152:155], v[24:27]\n\t"
      "v_mfma_f32_16x16x32_bf16 v[32:35], v[76:79], v[152:155], v[32:35]\n\t"
      "s_lshl_b32 s78, s73, 12\n\t"
      "s_add_u32 s78, s78, s93\n\t"
      "s_add_u32 s73, s73, 1\n\t"
      "s_cmp_eq_u32 s73, 7\n\t"
      "s_cselect_b32 s73, 0, s73\n\t"
      "buffer_load_dwordx4 v[192:195], v16, s[48:51], s78 offen offset:0 sc1\n\t"
      "v_mfma_f32_16x16x32_bf16 v[40:43], v[80:83], v[152:155], v[40:43]\n\t"
      "v_mfma_f32_16x16x32_bf16 v[48:51], v[84:87], v[152:155], v[48:51]\n\t"
      "s_waitcnt vmcnt(9)\n\t"
      "v_mfma_f32_16x16x32_bf16 v[28:31], v[72:75], v[156:159], v[28:31]\n\t"
      "v_mfma_f32_16x16x32_bf16 v[36:39], v[76:79], v[156:159], v[36:39]\n\t"
      "buffer_load_dwordx4 v[196:199], v16, s[48:51], s78 offen offset:1024 sc1\n\t"
      "v_mfma_f32_16x16x32_bf16 v[44:47], v[80:83], v[156:159], v[44:47]\n\t"
      "v_mfma_f32_16x16x32_bf16 v[52:55], v[84:87], v[156:159], v[52:55]\n\t"
      "s_waitcnt vmcnt(9)\n\t"
      "v_mfma_f32_16x16x32_bf16 v[24:27], v[88:91], v[160:163], v[24:27]\n\t"
      "s_barrier\n\t"
      "ds_read_b64_tr_b16 v[56:57], v10 offset:0\n\t"
      "v_mfma_f32_16x16x32_bf16 v[32:35], v[92:95], v[160:163], v[32:35]\n\t"
      "ds_read_b64_tr_b16 v[58:59], v10 offset:2048\n\t"
      "buffer_load_dwordx4 v[200:203], v16, s[48:51], s78 offen offset:2048 sc1\n\t"
      "v_mfma_f32_16x16x32_bf16 v[40:43], v[96:99], v[160:163], v[40:43]\n\t"
      "ds_read_b64_tr_b16 v[60:61], v11 offset:0\n\t"
      "v_mfma_f32_16x16x32_bf16 v[48:51], v[100:103], v[160:163], v[48:51]\n\t"
      "ds_read_b64_tr_b16 v[62:63], v11 offset:2048\n\t"
      "s_waitcnt vmcnt(9)\n\t"
      "v_mfma_f32_16x16x32_bf16 v[28:31], v[88:91], v[164:167], v[28:31]\n\t"
      "ds_read_b64_tr_b16 v[64:65], v12 offset:0\n\t"
      "v_mfma_f32_16x16x32_bf16 v[36:39], v[92:95], v[164:167], v[36:39]\n\t"
      "ds_read_b64_tr_b16 v[66:67], v12 offset:2048\n\t"
      "buffer_load_dwordx4 v[204:207], v16, s[48:51], s78 offen offset:3072 sc1\n\t"
      "v_mfma_f32_16x16x32_bf16 v[44:47], v[96:99], v[164:167], v[44:47]\n\t"
      "ds_read_b64_tr_b16 v[68:69], v13 offset:0\n\t"
      "v_mfma_f32_16x16x32_bf16 v[52:55], v[100:103], v[164:167], v[52:55]\n\t"
      "ds_read_b64_tr_b16 v[70:71], v13 offset:2048\n\t"
      "s_waitcnt vmcnt(9)\n\t"
      "v_mfma_f32_16x16x32_bf16 v[24:27], v[104:107], v[168:171], v[24:27]\n\t"
      "ds_read_b64_tr_b16 v[72:73], v10 offset:4096\n\t"
      "v_mfma_f32_16x16x32_bf16 v[32:35], v[108:111], v[168:171], v[32:35]\n\t"
      "ds_read_b64_tr_b16 v[74:75], v10 offset:6144\n\t"
      "s_lshl_b32 s78, s73, 12\n\t"
      "s_add_u32 s78, s78, s93\n\t"
      "s_add_u32 s73, s73, 1\n\t"
      "s_cmp_eq_u32 s73, 7\n\t"
      "s_cselect_b32 s73, 0, s73\n\t"
      "buffer_load_dwordx4 v[208:211], v16, s[48:51], s78 offen offset:0 sc1\n\t"
      "v_mfma_f32_16x16x32_bf16 v[40:43], v[112:115], v[168:171], v[40:43]\n\t"
      "ds_read_b64_tr_b16 v[76:77], v11 offset:4096\n\t"
      "v_mfma_f32_16x16x32_bf16 v[48:51], v[116:119], v[168:171], v[48:51]\n\t"
      "ds_read_b64_tr_b16 v[78:79], v11 offset:6144\n\t"
      "s_waitcnt vmcnt(9)\n\t"
      "v_mfma_f32_16x16x32_bf16 v[28:31], v[104:107], v[172:175], v[28:31]\n\t"
      "ds_read_b64_tr_b16 v[80:81], v12 offset:4096\n\t"
      "v_mfma_f32_16x16x32_bf16 v[36:39], v[108:111], v[172:175], v[36:39]\n\t"
      "ds_read_b64_tr_b16 v[82:83], v12 offset:6144\n\t"
      "buffer_load_dwordx4 v[212:215], v16, s[48:51], s78 offen offset:1024 sc1\n\t"
      "v_mfma_f32_16x16x32_bf16 v[44:47], v[112:115], v[172:175], v[44:47]\n\t"
      "ds_read_b64_tr_b16 v[84:85], v13 offset:4096\n\t"
      "v_mfma_f32_16x16x32_bf16 v[52:55], v[116:119], v[172:175], v[52:55]\n\t"
      "ds_read_b64_tr_b16 v[86:87], v13 offset:6144\n\t"
      "s_waitcnt vmcnt(9) lgkmcnt(8)\n\t"
      "v_mfma_f32_16x16x32_bf16 v[24:27], v[56:59], v[176:179], v[24:27]\n\t"
      "ds_read_b64_tr_b16 v[88:89], v10 offset:32768\n\t"
      "v_mfma_f32_16x16x32_bf16 v[32:35], v[60:63], v[176:179], v[32:35]\n\t"
      "ds_read_b64_tr_b16 v[90:91], v10 offset:34816\n\t"
      "buffer_load_dwordx4 v[216:219], v16, s[48:51], s78 offen offset:2048 sc1\n\t"
      "v_mfma_f32_16x16x32_bf16 v[40:43], v[64:67], v[176:179], v[40:43]\n\t"
      "ds_read_b64_tr_b16 v[92:93], v11 offset:32768\n\t"
      "v_mfma_f32_16x16x32_bf16 v[48:51], v[68:71], v[176:179], v[48:51]\n\t"
      "ds_read_b64_tr_b16 v[94:95], v11 offset:34816\n\t"
      "s_waitcnt vmcnt(9)\n\t"
      "v_mfma_f32_16x16x32_bf16 v[28:31], v[56:59], v[180:183], v[28:31]\n\t"
      "ds_read_b64_tr_b16 v[96:97], v12 offset:32768\n\t"
      "v_mfma_f32_16x16x32_bf16 v[36:39], v[60:63], v[180:183], v[36:39]\n\t"
      "ds_read_b64_tr_b16 v[98:99], v12 offset:34816\n\t"
      "buffer_load_dwordx4 v[220:223], v16, s[48:51], s78 offen offset:3072 sc1\n\t"
      "v_mfma_f32_16x16x32_bf16 v[44:47], v[64:67], v[180:183], v[44:47]\n\t"
      "ds_read_b64_tr_b16 v[100:101], v13 offset:32768\n\t"
      "v_mfma_f32_16x16x32_bf16 v[52:55], v[68:71], v[180:183], v[52:55]\n\t"
      "ds_read_b64_tr_b16 v[102:103], v13 offset:34816\n\t"
      "s_waitcnt vmcnt(9) lgkmcnt(8)\n\t"
      "v_mfma_f32_16x16x32_bf16 v[24:27], v[72:75], v[184:187], v[24:27]\n\t"
      "ds_read_b64_tr_b16 v[104:105], v10 offset:36864\n\t"
      "v_mfma_f32_16x16x32_bf16 v[32:35], v[76:79], v[184:187], v[32:35]\n\t"
      "ds_read_b64_tr_b16 v[106:107], v10 offset:38912\n\t"
      "s_lshl_b32 s78, s73, 12\n\t"
      "s_add_u32 s78, s78, s93\n\t"
      "s_add_u32 s73, s73, 1\n\t"
      "s_cmp_eq_u32 s73, 7\n\t"
      "s_cselect_b32 s73, 0, s73\n\t"
      "buffer_load_dwordx4 v[224:227], v16, s[48:51], s78 offen offset:0 sc1\n\t"
      "v_mfma_f32_16x16x32_bf16 v[40:43], v[80:83], v[184:187], v[40:43]\n\t"
      "ds_read_b64_tr_b16 v[108:109], v11 offset:36864\n\t"
      "v_mfma_f32_16x16x32_bf16 v[48:51], v[84:87], v[184:187], v[48:51]\n\t"
      "ds_read_b64_tr_b16 v[110:111], v11 offset:38912\n\t"
      "s_waitcnt vmcnt(9)\n\t"
      "v_mfma_f32_16x16x32_bf16 v[28:31], v[72:75], v[188:191], v[28:31]\n\t"
      "ds_read_b64_tr_b16 v[112:113], v12 offset:36864\n\t"
      "v_mfma_f32_16x16x32_bf16 v[36:39], v[76:79], v[188:191], v[36:39]\n\t"
      "ds_read_b64_tr_b16 v[114:115], v12 offset:38912\n\t"
      "buffer_load_dwordx4 v[228:231], v16, s[48:51], s78 offen offset:1024 sc1\n\t"
      "v_mfma_f32_16x16x32_bf16 v[44:47], v[80:83], v[188:191], v[44:47]\n\t"
      "ds_read_b64_tr_b16 v[116:117], v13 offset:36864\n\t"
      "v_mfma_f32_16x16x32_bf16 v[52:55], v[84:87], v[188:191], v[52:55]\n\t"
      "ds_read_b64_tr_b16 v[118:119], v13 offset:38912\n\t"
      "v_add_u32 v10, 0x10000, v10\n\t"
      "v_add_u32 v11, 0x10000, v11\n\t"
      "v_add_u32 v12, 0x10000, v12\n\t"
      "v_add_u32 v13, 0x10000, v13\n\t"
      "s_waitcnt vmcnt(9) lgkmcnt(8)\n\t"
      "v_mfma_f32_16x16x32_bf16 v[24:27], v[88:91], v[192:195], v[24:27]\n\t"
      "ds_read_b64_tr_b16 v[56:57], v10 offset:0\n\t"
      "v_mfma_f32_16x16x32_bf16 v[32:35], v[92:95], v[192:195], v[32:35]\n\t"
      "ds_read_b64_tr_b16 v[58:59], v10 offset:2048\n\t"
      "buffer_load_dwordx4 v[232:235], v16, s[48:51], s78 offen offset:2048 sc1\n\t"
      "v_mfma_f32_16x16x32_bf16 v[40:43], v[96:99], v[192:195], v[40:43]\n\t"
      "ds_read_b64_tr_b16 v[60:61], v11 offset:0\n\t"
      "v_mfma_f32_16x16x32_bf16 v[48:51], v[100:103], v[192:195], v[48:51]\n\t"
      "ds_read_b64_tr_b16 v[62:63], v11 offset:2048\n\t"
      "s_waitcnt vmcnt(9)\n\t"
      "v_mfma_f32_16x16x32_bf16 v[28:31], v[88:91], v[196:199], v[28:31]\n\t"
      "ds_read_b64_tr_b16 v[64:65], v12 offset:0\n\t"
      "v_mfma_f32_16x16x32_bf16 v[36:39], v[92:95], v[196:199], v[36:39]\n\t"
      "ds_read_b64_tr_b16 v[66:67], v12 offset:2048\n\t"
      "buffer_load_dwordx4 v[236:239], v16, s[48:51], s78 offen offset:3072 sc1\n\t"
      "v_mfma_f32_16x16x32_bf16 v[44:47], v[96:99], v[196:199], v[44:47]\n\t"
      "ds_read_b64_tr_b16 v[68:69], v13 offset:0\n\t"
      "v_mfma_f32_16x16x32_bf16 v[52:55], v[100:103], v[196:199], v[52:55]\n\t"
      "ds_read_b64_tr_b16 v[70:71], v13 offset:2048\n\t"
      "s_waitcnt vmcnt(9) lgkmcnt(8)\n\t"
      "v_mfma_f32_16x16x32_bf16 v[24:27], v[104:107], v[200:203], v[24:27]\n\t"
      "ds_read_b64_tr_b16 v[72:73], v10 offset:4096\n\t"
      "v_mfma_f32_16x16x32_bf16 v[32:35], v[108:111], v[200:203], v[32:35]\n\t"
      "ds_read_b64_tr_b16 v[74:75], v10 offset:6144\n\t"
      "s_lshl_b32 s78, s73, 12\n\t"
      "s_add_u32 s78, s78, s93\n\t"
      "s_add_u32 s73, s73, 1\n\t"
      "s_cmp_eq_u32 s73, 7\n\t"
      "s_cselect_b32 s73, 0, s73\n\t"
      "buffer_load_dwordx4 v[240:243], v16, s[48:51], s78 offen offset:0 sc1\n\t"
      "v_mfma_f32_16x16x32_bf16 v[40:43], v[112:115], v[200:203], v[40:43]\n\t"
      "ds_read_b64_tr_b16 v[76:77], v11 offset:4096\n\t"
      "v_mfma_f32_16x16x32_bf16 v[48:51], v[116:119], v[200:203], v[48:51]\n\t"
      "ds_read_b64_tr_b16 v[78:79], v11 offset:6144\n\t"
      "s_waitcnt vmcnt(9)\n\t"
      "v_mfma_f32_16x16x32_bf16 v[28:31], v[104:107], v[204:207], v[28:31]\n\t"
      "ds_read_b64_tr_b16 v[80:81], v12 offset:4096\n\t"
      "v_mfma_f32_16x16x32_bf16 v[36:39], v[108:111], v[204:207], v[36:39]\n\t"
      "ds_read_b64_tr_b16 v[82:83], v12 offset:6144\n\t"
      "buffer_load_dwordx4 v[244:247], v16, s[48:51], s78 offen offset:1024 sc1\n\t"
      "v_mfma_f32_16x16x32_bf16 v[44:47], v[112:115], v[204:207], v[44:47]\n\t"
      "ds_read_b64_tr_b16 v[84:85], v13 offset:4096\n\t"
      "v_mfma_f32_16x16x32_bf16 v[52:55], v[116:119], v[204:207], v[52:55]\n\t"
      "ds_read_b64_tr_b16 v[86:87], v13 offset:6144\n\t"
      "s_waitcnt vmcnt(9) lgkmcnt(8)\n\t"
      "v_mfma_f32_16x16x32_bf16 v[24:27], v[56:59], v[208:211], v[24:27]\n\t"
      "s_barrier\n\t"
      "ds_read_b64_tr_b16 v[88:89], v10 offset:32768\n\t"
      "v_mfma_f32_16x16x32_bf16 v[32:35], v[60:63], v[208:211], v[32:35]\n\t"
      "ds_read_b64_tr_b16 v[90:91], v10 offset:34816\n\t"
      "buffer_load_dwordx4 v[248:251], v16, s[48:51], s78 offen offset:2048 sc1\n\t"
      "v_mfma_f32_16x16x32_bf16 v[40:43], v[64:67], v[208:211], v[40:43]\n\t"
      "ds_read_b64_tr_b16 v[92:93], v11 offset:32768\n\t"
      "v_mfma_f32_16x16x32_bf16 v[48:51], v[68:71], v[208:211], v[48:51]\n\t"
      "ds_read_b64_tr_b16 v[94:95], v11 offset:34816\n\t"
      "s_waitcnt vmcnt(9)\n\t"
      "v_mfma_f32_16x16x32_bf16 v[28:31], v[56:59], v[212:215], v[28:31]\n\t"
      "ds_read_b64_tr_b16 v[96:97], v12 offset:32768\n\t"
      "v_mfma_f32_16x16x32_bf16 v[36:39], v[60:63], v[212:215], v[36:39]\n\t"
      "ds_read_b64_tr_b16 v[98:99], v12 offset:34816\n\t"
      "buffer_load_dwordx4 v[252:255], v16, s[48:51], s78 offen offset:3072 sc1\n\t"
      "v_mfma_f32_16x16x32_bf16 v[44:47], v[64:67], v[212:215], v[44:47]\n\t"
      "ds_read_b64_tr_b16 v[100:101], v13 offset:32768\n\t"
      "v_mfma_f32_16x16x32_bf16 v[52:55], v[68:71], v[212:215], v[52:55]\n\t"
      "ds_read_b64_tr_b16 v[102:103], v13 offset:34816\n\t"
      "s_waitcnt vmcnt(9) lgkmcnt(8)\n\t"
      "v_mfma_f32_16x16x32_bf16 v[24:27], v[72:75], v[216:219], v[24:27]\n\t"
      "ds_read_b64_tr_b16 v[104:105], v10 offset:36864\n\t"
      "v_mfma_f32_16x16x32_bf16 v[32:35], v[76:79], v[216:219], v[32:35]\n\t"
      "ds_read_b64_tr_b16 v[106:107], v10 offset:38912\n\t"
      "v_mfma_f32_16x16x32_bf16 v[40:43], v[80:83], v[216:219], v[40:43]\n\t"
      "ds_read_b64_tr_b16 v[108:109], v11 offset:36864\n\t"
      "v_mfma_f32_16x16x32_bf16 v[48:51], v[84:87], v[216:219], v[48:51]\n\t"
      "ds_read_b64_tr_b16 v[110:111], v11 offset:38912\n\t"
      "s_waitcnt vmcnt(8)\n\t"
      "v_mfma_f32_16x16x32_bf16 v[28:31], v[72:75], v[220:223], v[28:31]\n\t"
      "ds_read_b64_tr_b16 v[112:113], v12 offset:36864\n\t"
      "v_mfma_f32_16x16x32_bf16 v[36:39], v[76:79], v[220:223], v[36:39]\n\t"
      "ds_read_b64_tr_b16 v[114:115], v12 offset:38912\n\t"
      "v_mfma_f32_16x16x32_bf16 v[44:47], v[80:83], v[220:223], v[44:47]\n\t"
      "ds_read_b64_tr_b16 v[116:117], v13 offset:36864\n\t"
      "v_mfma_f32_16x16x32_bf16 v[52:55], v[84:87], v[220:223], v[52:55]\n\t"
      "ds_read_b64_tr_b16 v[118:119], v13 offset:38912\n\t"
      "v_add_u32 v10, 0x10000, v10\n\t"
      "v_add_u32 v11, 0x10000, v11\n\t"
      "v_add_u32 v12, 0x10000, v12\n\t"
      "v_add_u32 v13, 0x10000, v13\n\t"
      "s_waitcnt vmcnt(7) lgkmcnt(8)\n\t"
      "v_mfma_f32_16x16x32_bf16 v[24:27], v[88:91], v[224:227], v[24:27]\n\t"
      "ds_read_b64_tr_b16 v[56:57], v10 offset:0\n\t"
      "v_mfma_f32_16x16x32_bf16 v[32:35], v[92:95], v[224:227], v[32:35]\n\t"
      "ds_read_b64_tr_b16 v[58:59], v10 offset:2048\n\t"
      "v_mfma_f32_16x16x32_bf16 v[40:43], v[96:99], v[224:227], v[40:43]\n\t"
      "ds_read_b64_tr_b16 v[60:61], v11 offset:0\n\t"
      "v_mfma_f32_16x16x32_bf16 v[48:51], v[100:103], v[224:227], v[48:51]\n\t"
      "ds_read_b64_tr_b16 v[62:63], v11 offset:2048\n\t"
      "s_waitcnt vmcnt(6)\n\t"
      "v_mfma_f32_16x16x32_bf16 v[28:31], v[88:91], v[228:231], v[28:31]\n\t"
      "ds_read_b64_tr_b16 v[64:65], v12 offset:0\n\t"
      "v_mfma_f32_16x16x32_bf16 v[36:39], v[92:95], v[228:231], v[36:39]\n\t"
      "ds_read_b64_tr_b16 v[66:67], v12 offset:2048\n\t"
      "v_mfma_f32_16x16x32_bf16 v[44:47], v[96:99], v[228:231], v[44:47]\n\t"
      "ds_read_b64_tr_b16 v[68:69], v13 offset:0\n\t"
      "v_mfma_f32_16x16x32_bf16 v[52:55], v[100:103], v[228:231], v[52:55]\n\t"
      "ds_read_b64_tr_b16 v[70:71], v13 offset:2048\n\t"
      "s_waitcnt vmcnt(5) lgkmcnt(8)\n\t"
      "v_mfma_f32_16x16x32_bf16 v[24:27], v[104:107], v[232:235], v[24:27]\n\t"
      "ds_read_b64_tr_b16 v[72:73], v10 offset:4096\n\t"
      "v_mfma_f32_16x16x32_bf16 v[32:35], v[108:111], v[232:235], v[32:35]\n\t"
      "ds_read_b64_tr_b16 v[74:75], v10 offset:6144\n\t"
      "v_mfma_f32_16x16x32_bf16 v[40:43], v[112:115], v[232:235], v[40:43]\n\t"
      "ds_read_b64_tr_b16 v[76:77], v11 offset:4096\n\t"
      "v_mfma_f32_16x16x32_bf16 v[48:51], v[116:119], v[232:235], v[48:51]\n\t"
      "ds_read_b64_tr_b16 v[78:79], v11 offset:6144\n\t"
      "s_waitcnt vmcnt(4)\n\t"
      "v_mfma_f32_16x16x32_bf16 v[28:31], v[104:107], v[236:239], v[28:31]\n\t"
      "ds_read_b64_tr_b16 v[80:81], v12 offset:4096\n\t"
      "v_mfma_f32_16x16x32_bf16 v[36:39], v[108:111], v[236:239], v[36:39]\n\t"
      "ds_read_b64_tr_b16 v[82:83], v12 offset:6144\n\t"
      "v_mfma_f32_16x16x32_bf16 v[44:47], v[112:115], v[236:239], v[44:47]\n\t"
      "ds_read_b64_tr_b16 v[84:85], v13 offset:4096\n\t"
      "v_mfma_f32_16x16x32_bf16 v[52:55], v[116:119], v[236:239], v[52:55]\n\t"
      "ds_read_b64_tr_b16 v[86:87], v13 offset:6144\n\t"
      "s_waitcnt vmcnt(3) lgkmcnt(8)\n\t"
      "v_mfma_f32_16x16x32_bf16 v[24:27], v[56:59], v[240:243], v[24:27]\n\t"
      "v_mfma_f32_16x16x32_bf16 v[32:35], v[60:63], v[240:243], v[32:35]\n\t"
      "v_mfma_f32_16x16x32_bf16 v[40:43], v[64:67], v[240:243], v[40:43]\n\t"
      "v_mfma_f32_16x16x32_bf16 v[48:51], v[68:71], v[240:243], v[48:51]\n\t"
      "s_waitcnt vmcnt(2)\n\t"
      "v_mfma_f32_16x16x32_bf16 v[28:31], v[56:59], v[244:247], v[28:31]\n\t"
      "v_mfma_f32_16x16x32_bf16 v[36:39], v[60:63], v[244:247], v[36:39]\n\t"
      "v_mfma_f32_16x16x32_bf16 v[44:47], v[64:67], v[244:247], v[44:47]\n\t"
      "v_mfma_f32_16x16x32_bf16 v[52:55], v[68:71], v[244:247], v[52:55]\n\t"
      "s_waitcnt vmcnt(1) lgkmcnt(0)\n\t"
      "v_mfma_f32_16x16x32_bf16 v[24:27], v[72:75], v[248:251], v[24:27]\n\t"
      "v_mfma_f32_16x16x32_bf16 v[32:35], v[76:79], v[248:251], v[32:35]\n\t"
      "v_mfma_f32_16x16x32_bf16 v[40:43], v[80:83], v[248:251], v[40:43]\n\t"
      "v_mfma_f32_16x16x32_bf16 v[48:51], v[84:87], v[248:251], v[48:51]\n\t"
      "s_waitcnt vmcnt(0)\n\t"
      "v_mfma_f32_16x16x32_bf16 v[28:31], v[72:75], v[252:255], v[28:31]\n\t"
      "v_mfma_f32_16x16x32_bf16 v[36:39], v[76:79], v[252:255], v[36:39]\n\t"
      "v_mfma_f32_16x16x32_bf16 v[44:47], v[80:83], v[252:255], v[44:47]\n\t"
      "v_mfma_f32_16x16x32_bf16 v[52:55], v[84:87], v[252:255], v[52:55]\n\t"
      "s_nop 11\n\t"
      "ds_write_b128 v2, v[24:27] offset:0\n\t"
      "ds_write_b128 v3, v[32:35] offset:0\n\t"
      "ds_write_b128 v2, v[40:43] offset:128\n\t"
      "ds_write_b128 v3, v[48:51] offset:128\n\t"
      "ds_write_b128 v2, v[28:31] offset:4096\n\t"
      "ds_write_b128 v3, v[36:39] offset:4096\n\t"
      "ds_write_b128 v2, v[44:47] offset:4224\n\t"
      "ds_write_b128 v3, v[52:55] offset:4224\n\t"
      "s_waitcnt lgkmcnt(0)\n\t"
      "s_barrier\n\t"
      "ds_read_b128 v[56:59], v7 offset:0\n\t"
      "ds_read_b128 v[60:63], v8 offset:0\n\t"
      "ds_read_b128 v[64:67], v7 offset:8192\n\t"
      "ds_read_b128 v[68:71], v8 offset:8192\n\t"
      "ds_read_b128 v[72:75], v7 offset:16384\n\t"
      "ds_read_b128 v[76:79], v8 offset:16384\n\t"
      "ds_read_b128 v[80:83], v7 offset:24576\n\t"
      "ds_read_b128 v[84:87], v8 offset:24576\n\t"
      "s_waitcnt lgkmcnt(4)\n\t"
      "v_pk_add_f32 v[56:57], v[56:57], v[64:65]\n\t"
      "v_pk_add_f32 v[58:59], v[58:59], v[66:67]\n\t"
      "v_pk_add_f32 v[60:61], v[60:61], v[68:69]\n\t"
      "v_pk_add_f32 v[62:63], v[62:63], v[70:71]\n\t"
      "s_waitcnt lgkmcnt(2)\n\t"
      "v_pk_add_f32 v[56:57], v[56:57], v[72:73]\n\t"
      "v_pk_add_f32 v[58:59], v[58:59], v[74:75]\n\t"
      "v_pk_add_f32 v[60:61], v[60:61], v[76:77]\n\t"
      "v_pk_add_f32 v[62:63], v[62:63], v[78:79]\n\t"
      "s_waitcnt lgkmcnt(0)\n\t"
      "v_pk_add_f32 v[56:57], v[56:57], v[80:81]\n\t"
      "v_pk_add_f32 v[58:59], v[58:59], v[82:83]\n\t"
      "v_pk_add_f32 v[60:61], v[60:61], v[84:85]\n\t"
      "v_pk_add_f32 v[62:63], v[62:63], v[86:87]\n\t"
      "v_cvt_pk_bf16_f32 v88, v56, v57\n\t"
      "v_cvt_pk_bf16_f32 v89, v58, v59\n\t"
      "v_cvt_pk_bf16_f32 v90, v60, v61\n\t"
      "v_cvt_pk_bf16_f32 v91, v62, v63\n\t"
      "buffer_store_dwordx4 v[88:91], v9, s[44:47], s59 offen \n\t"
      "s_endpgm\n\t"
      ".Lend%=:\n\t"
      :
      : "s"(__builtin_amdgcn_kernarg_segment_ptr()), "s"(blockIdx.x), "v"(threadIdx.x)
      : "memory", "vcc", "scc", "s16", "s17", "s18", "s19", "s20", "s21", "s22", "s23", "s24", "s25", "s26", "s27", "s28", "s29", "s30", "s31", "s36", "s37", "s38", "s39", "s40", "s41", "s42", "s43", "s44", "s45", "s46", "s47", "s48", "s49", "s50", "s51", "s52", "s53", "s54", "s55", "s56", "s57", "s58", "s59", "s60", "s61", "s62", "s63", "s64", "s65", "s66", "s67", "s68", "s69", "s70", "s71", "s72", "s73", "s74", "s75", "s76", "s77", "s78", "s79", "s80", "s81", "s82", "s83", "s84", "s85", "s86", "s87", "s88", "s89", "s90", "s91", "s92", "s93", "s94", "s95", "s96", "s97", "s98", "s99", "v1", "v2", "v3", "v4", "v5", "v6", "v7", "v8", "v9", "v10", "v11", "v12", "v13", "v14", "v15", "v16", "v17", "v18", "v19", "v20", "v21", "v22", "v23", "v24", "v25", "v26", "v27", "v28", "v29", "v30", "v31", "v32", "v33", "v34", "v35", "v36", "v37", "v38", "v39", "v40", "v41", "v42", "v43", "v44", "v45", "v46", "v47", "v48", "v49", "v50", "v51", "v52", "v53", "v54", "v55", "v56", "v57", "v58", "v59", "v60", "v61", "v62", "v63", "v64", "v65", "v66", "v67", "v68", "v69", "v70", "v71", "v72", "v73", "v74", "v75", "v76", "v77", "v78", "v79", "v80", "v81", "v82", "v83", "v84", "v85", "v86", "v87", "v88", "v89", "v90", "v91", "v92", "v93", "v94", "v95", "v96", "v97", "v98", "v99", "v100", "v101", "v102", "v103", "v104", "v105", "v106", "v107", "v108", "v109", "v110", "v111", "v112", "v113", "v114", "v115", "v116", "v117", "v118", "v119", "v120", "v121", "v122", "v123", "v124", "v125", "v126", "v127", "v128", "v129", "v130", "v131", "v132", "v133", "v134", "v135", "v136", "v137", "v138", "v139", "v140", "v141", "v142", "v143", "v144", "v145", "v146", "v147", "v148", "v149", "v150", "v151", "v152", "v153", "v154", "v155", "v156", "v157", "v158", "v159", "v160", "v161", "v162", "v163", "v164", "v165", "v166", "v167", "v168", "v169", "v170", "v171", "v172", "v173", "v174", "v175", "v176", "v177", "v178", "v179", "v180", "v181", "v182", "v183", "v184", "v185", "v186", "v187", "v188", "v189", "v190", "v191", "v192", "v193", "v194", "v195", "v196", "v197", "v198", "v199", "v200", "v201", "v202", "v203", "v204", "v205", "v206", "v207", "v208", "v209", "v210", "v211", "v212", "v213", "v214", "v215", "v216", "v217", "v218", "v219", "v220", "v221", "v222", "v223", "v224", "v225", "v226", "v227", "v228", "v229", "v230", "v231", "v232", "v233", "v234", "v235", "v236", "v237", "v238", "v239", "v240", "v241", "v242", "v243", "v244", "v245", "v246", "v247", "v248", "v249", "v250", "v251", "v252", "v253", "v254", "v255");
}

extern "C" void kernel_launch(void* const* d_in, const int* in_sizes, int n_in, void* d_out, int out_size, void* d_ws, size_t ws_size,
                              hipStream_t stream) {
  (void)hipFuncSetAttribute((const void*)swiglu_fused, hipFuncAttributeMaxDynamicSharedMemorySize, 163840);
  swiglu_fused<<<256, 512, 163840, stream>>>(d_in[0], d_in[1], d_in[2], d_in[3], d_out, d_ws);
}
```
